```python
import torch

def _softmax(x, axis=-1):
    return torch.softmax(x, dim=axis)

def _gelu(x):
    return torch.nn.functional.gelu(x, approximate='tanh')

def _silu(x):
    return torch.nn.functional.silu(x)

def _sigmoid(x):
    return torch.sigmoid(x)

def _layernorm(x, axis=-1, eps=1e-05):
    m = x.mean(dim=axis, keepdim=True)
    v = x.var(dim=axis, keepdim=True, unbiased=False)
    return (x - m) / torch.sqrt(v + eps)

def _scatter_add(tgt, idx, src, axis=0):
    return tgt.clone().index_add_(axis, idx, src)

def _segment_sum(src, seg, n):
    out = torch.zeros((n,) + src.shape[1:], dtype=src.dtype)
    return out.index_add_(0, seg, src)

def _pad(x, pw):
    flat = tuple((v for pair in reversed(pw) for v in pair))
    return torch.nn.functional.pad(x, flat)
kernel_name = 'adaLN_cond__spectral_gfnet__moe_arch_mod__prompt'

def setup_inputs():

    _w = lambda a: (lambda t: t.to(torch.bfloat16) if t.is_floating_point() else t)(torch.as_tensor(a))

    def _A_setup():
        B, S, D = (1, 2048, 640)
        Df = D // 2 + 1
        return {'x': _w((lambda *s: torch.randn(*s, dtype=torch.bfloat16) * 0.05)(B, S, D)), 'w_r': _w((lambda *s: torch.randn(*s, dtype=torch.bfloat16) * 3.2)(S, Df) * 0.02), 'w_i': _w((lambda *s: torch.randn(*s, dtype=torch.bfloat16) * 3.2)(S, Df) * 0.02)}
    _A_in = _A_setup()

    def _B_setup():
        B, S, D, F, C = (1, 2048, 640, 1728, 8)
        return {'x': _w((lambda *s: torch.randn(*s, dtype=torch.bfloat16) * 0.05)(B, S, D)), 'w_r': _w((lambda *s: torch.randn(*s, dtype=torch.bfloat16) * 0.2)(D)), 'w_gate': _w((lambda *s: torch.randn(*s, dtype=torch.bfloat16) * 0.2)(D, F)), 'w_up': _w((lambda *s: torch.randn(*s, dtype=torch.bfloat16) * 0.2)(D, F)), 'w_down': _w((lambda *s: torch.randn(*s, dtype=torch.bfloat16) * 0.2)(F, D))}
    _B_in = _B_setup()
    return {'A_x': _A_in['x'], 'A_w_r': _A_in['w_r'], 'A_w_i': _A_in['w_i'], 'B_x': _B_in['x'], 'B_w_r': _B_in['w_r'], 'B_w_gate': _B_in['w_gate'], 'B_w_up': _B_in['w_up'], 'B_w_down': _B_in['w_down'], 'c': _w((lambda *s: torch.randn(*s, dtype=torch.bfloat16) * 0.1)(1, 640) * 8.0), 'w_mod': _w((lambda *s: torch.randn(*s, dtype=torch.bfloat16) * 0.1)(640, 6 * 640) * 640 ** (-0.5) * 8.0)}

def reference(A_x, A_w_r, A_w_i, B_x, B_w_r, B_w_gate, B_w_up, B_w_down, c, w_mod):

    def _A_ref(x, w_r, w_i):
        B, S, D = x.shape
        X = (lambda x, axes=(-2, -1): torch.fft.rfft2(x.float(), dim=axes))(x, axes=(-2, -1))
        W = w_r + 1j * w_i
        X = X * W
        y = (lambda x, s, axes=(-2, -1): torch.fft.irfft2(x, s=s, dim=axes).to(torch.bfloat16))(X, (S, D), axes=(-2, -1))
        return y

    def _B_ref(x, w_r, w_gate, w_up, w_down):
        B, S, D = x.shape
        C = 8
        r = x @ w_r
        idx = torch.argsort(-r, axis=-1)[:, :C]
        idx_e = (lambda x, n, axis: torch.repeat_interleave(x, n, dim=axis))(idx.reshape(B, C, 1), D, axis=2)
        x_sel = (lambda x, i, axis: torch.take_along_dim(x, i, dim=axis))(x, idx_e, axis=1)
        g = _silu(x_sel @ w_gate)
        u = x_sel @ w_up
        y = g * u @ w_down
        r_sel = _sigmoid((lambda x, i, axis: torch.take_along_dim(x, i, dim=axis))(r, idx, axis=1))
        y = y * r_sel.reshape(B, C, 1)
        b_off = (torch.arange(B) * S).reshape(B, 1)
        flat_idx = (idx + b_off).reshape(B * C)
        zero = (x * 0.0).reshape(B * S, D)
        upd = _scatter_add(zero, flat_idx, y.reshape(B * C, D), axis=0)
        return x + upd.reshape(B, S, D)
    x = A_x
    mod = (_silu(c) @ w_mod).reshape(c.shape[0], 6, 1, -1)
    sa, ca, ga, sb, cb, gb = (mod[:, 0].clone(), mod[:, 1].clone(), mod[:, 2].clone(), mod[:, 3].clone(), mod[:, 4].clone(), mod[:, 5].clone())
    h = _layernorm(x, axis=-1) * (1 + ca) + sa
    x = x + ga * _A_ref(h, A_w_r, A_w_i)
    h = _layernorm(x, axis=-1) * (1 + cb) + sb
    x = x + gb * _B_ref(h, B_w_r, B_w_gate, B_w_up, B_w_down)
    return x
```

```cpp
#include <hip/hip_runtime.h>
#include <hip/hip_bf16.h>
#include <stdint.h>

typedef float c32 __attribute__((ext_vector_type(2)));
typedef float f32x4 __attribute__((ext_vector_type(4)));
typedef unsigned u32x2 __attribute__((ext_vector_type(2)));
typedef unsigned u32x4 __attribute__((ext_vector_type(4)));
typedef __bf16 bf16x2_t __attribute__((ext_vector_type(2)));
typedef __bf16 bf16x8_t __attribute__((ext_vector_type(8)));
typedef short s16x4 __attribute__((ext_vector_type(4)));
typedef unsigned short u16;
#define LAS __attribute__((address_space(3)))

static __device__ const __attribute__((aligned(128))) float TWV[1760] = {
1.000000000e+00f,0.000000000e+00f,9.999517798e-01f,-9.817318991e-03f,9.998072386e-01f,-1.963369176e-02f,9.995663166e-01f,-2.944817394e-02f,
9.992290139e-01f,-3.925981745e-02f,9.987954497e-01f,-4.906767607e-02f,9.982656240e-01f,-5.887080356e-02f,9.976395369e-01f,-6.866826117e-02f,
9.969173074e-01f,-7.845909894e-02f,9.960990548e-01f,-8.824236691e-02f,9.951847196e-01f,-9.801714122e-02f,9.941744804e-01f,-1.077824607e-01f,
9.930684566e-01f,-1.175373942e-01f,9.918667078e-01f,-1.272810102e-01f,9.905693531e-01f,-1.370123476e-01f,9.891765118e-01f,-1.467304677e-01f,
9.876883626e-01f,-1.564344615e-01f,9.861049652e-01f,-1.661233753e-01f,9.844265580e-01f,-1.757962853e-01f,9.826532602e-01f,-1.854522377e-01f,
9.807852507e-01f,-1.950903237e-01f,9.788227677e-01f,-2.047096044e-01f,9.767658710e-01f,-2.143091559e-01f,9.746148586e-01f,-2.238880545e-01f,
9.723699093e-01f,-2.334453613e-01f,9.700312614e-01f,-2.429801822e-01f,9.675990939e-01f,-2.524915636e-01f,9.650736451e-01f,-2.619786263e-01f,
9.624552131e-01f,-2.714404464e-01f,9.597440362e-01f,-2.808761001e-01f,9.569403529e-01f,-2.902846634e-01f,9.540444016e-01f,-2.996652722e-01f,
9.510565400e-01f,-3.090170026e-01f,9.479769468e-01f,-3.183389306e-01f,9.448060393e-01f,-3.276301920e-01f,9.415440559e-01f,-3.368898630e-01f,
9.381913543e-01f,-3.461170495e-01f,9.347481728e-01f,-3.553108871e-01f,9.312149286e-01f,-3.644705117e-01f,9.275919199e-01f,-3.735949695e-01f,
1.000000000e+00f,0.000000000e+00f,9.998072386e-01f,-1.963369176e-02f,9.992290139e-01f,-3.925981745e-02f,9.982656240e-01f,-5.887080356e-02f,
9.969173074e-01f,-7.845909894e-02f,9.951847196e-01f,-9.801714122e-02f,9.930684566e-01f,-1.175373942e-01f,9.905693531e-01f,-1.370123476e-01f,
9.876883626e-01f,-1.564344615e-01f,9.844265580e-01f,-1.757962853e-01f,9.807852507e-01f,-1.950903237e-01f,9.767658710e-01f,-2.143091559e-01f,
9.723699093e-01f,-2.334453613e-01f,9.675990939e-01f,-2.524915636e-01f,9.624552131e-01f,-2.714404464e-01f,9.569403529e-01f,-2.902846634e-01f,
9.510565400e-01f,-3.090170026e-01f,9.448060393e-01f,-3.276301920e-01f,9.381913543e-01f,-3.461170495e-01f,9.312149286e-01f,-3.644705117e-01f,
9.238795042e-01f,-3.826834261e-01f,9.161879420e-01f,-4.007488191e-01f,9.081431627e-01f,-4.186597466e-01f,8.997482657e-01f,-4.364092350e-01f,
8.910065293e-01f,-4.539904892e-01f,8.819212914e-01f,-4.713967443e-01f,8.724960089e-01f,-4.886212349e-01f,8.627343774e-01f,-5.056573749e-01f,
8.526401520e-01f,-5.224985480e-01f,8.422172070e-01f,-5.391383171e-01f,8.314695954e-01f,-5.555702448e-01f,8.204014301e-01f,-5.717879534e-01f,
8.090170026e-01f,-5.877852440e-01f,7.973206639e-01f,-6.035559177e-01f,7.853169441e-01f,-6.190939546e-01f,7.730104327e-01f,-6.343932748e-01f,
7.604059577e-01f,-6.494480371e-01f,7.475083470e-01f,-6.642524600e-01f,7.343224883e-01f,-6.788007617e-01f,7.208535671e-01f,-6.930873394e-01f,
1.000000000e+00f,0.000000000e+00f,9.995663166e-01f,-2.944817394e-02f,9.982656240e-01f,-5.887080356e-02f,9.960990548e-01f,-8.824236691e-02f,
9.930684566e-01f,-1.175373942e-01f,9.891765118e-01f,-1.467304677e-01f,9.844265580e-01f,-1.757962853e-01f,9.788227677e-01f,-2.047096044e-01f,
9.723699093e-01f,-2.334453613e-01f,9.650736451e-01f,-2.619786263e-01f,9.569403529e-01f,-2.902846634e-01f,9.479769468e-01f,-3.183389306e-01f,
9.381913543e-01f,-3.461170495e-01f,9.275919199e-01f,-3.735949695e-01f,9.161879420e-01f,-4.007488191e-01f,9.039893150e-01f,-4.275550842e-01f,
8.910065293e-01f,-4.539904892e-01f,8.772509098e-01f,-4.800321162e-01f,8.627343774e-01f,-5.056573749e-01f,8.474695683e-01f,-5.308440328e-01f,
8.314695954e-01f,-5.555702448e-01f,8.147484660e-01f,-5.798145533e-01f,7.973206639e-01f,-6.035559177e-01f,7.792012691e-01f,-6.267738342e-01f,
7.604059577e-01f,-6.494480371e-01f,7.409511209e-01f,-6.715589762e-01f,7.208535671e-01f,-6.930873394e-01f,7.001308203e-01f,-7.140145898e-01f,
6.788007617e-01f,-7.343224883e-01f,6.568819284e-01f,-7.539935112e-01f,6.343932748e-01f,-7.730104327e-01f,6.113544106e-01f,-7.913569212e-01f,
5.877852440e-01f,-8.090170026e-01f,5.637062788e-01f,-8.259753585e-01f,5.391383171e-01f,-8.422172070e-01f,5.141027570e-01f,-8.577286005e-01f,
4.886212349e-01f,-8.724960089e-01f,4.627159238e-01f,-8.865066171e-01f,4.364092350e-01f,-8.997482657e-01f,4.097240269e-01f,-9.122095108e-01f,
1.000000000e+00f,0.000000000e+00f,9.992290139e-01f,-3.925981745e-02f,9.969173074e-01f,-7.845909894e-02f,9.930684566e-01f,-1.175373942e-01f,
9.876883626e-01f,-1.564344615e-01f,9.807852507e-01f,-1.950903237e-01f,9.723699093e-01f,-2.334453613e-01f,9.624552131e-01f,-2.714404464e-01f,
9.510565400e-01f,-3.090170026e-01f,9.381913543e-01f,-3.461170495e-01f,9.238795042e-01f,-3.826834261e-01f,9.081431627e-01f,-4.186597466e-01f,
8.910065293e-01f,-4.539904892e-01f,8.724960089e-01f,-4.886212349e-01f,8.526401520e-01f,-5.224985480e-01f,8.314695954e-01f,-5.555702448e-01f,
8.090170026e-01f,-5.877852440e-01f,7.853169441e-01f,-6.190939546e-01f,7.604059577e-01f,-6.494480371e-01f,7.343224883e-01f,-6.788007617e-01f,
7.071067691e-01f,-7.071067691e-01f,6.788007617e-01f,-7.343224883e-01f,6.494480371e-01f,-7.604059577e-01f,6.190939546e-01f,-7.853169441e-01f,
5.877852440e-01f,-8.090170026e-01f,5.555702448e-01f,-8.314695954e-01f,5.224985480e-01f,-8.526401520e-01f,4.886212349e-01f,-8.724960089e-01f,
4.539904892e-01f,-8.910065293e-01f,4.186597466e-01f,-9.081431627e-01f,3.826834261e-01f,-9.238795042e-01f,3.461170495e-01f,-9.381913543e-01f,
3.090170026e-01f,-9.510565400e-01f,2.714404464e-01f,-9.624552131e-01f,2.334453613e-01f,-9.723699093e-01f,1.950903237e-01f,-9.807852507e-01f,
1.564344615e-01f,-9.876883626e-01f,1.175373942e-01f,-9.930684566e-01f,7.845909894e-02f,-9.969173074e-01f,3.925981745e-02f,-9.992290139e-01f,
1.000000000e+00f,0.000000000e+00f,9.987954497e-01f,-4.906767607e-02f,9.951847196e-01f,-9.801714122e-02f,9.891765118e-01f,-1.467304677e-01f,
9.807852507e-01f,-1.950903237e-01f,9.700312614e-01f,-2.429801822e-01f,9.569403529e-01f,-2.902846634e-01f,9.415440559e-01f,-3.368898630e-01f,
9.238795042e-01f,-3.826834261e-01f,9.039893150e-01f,-4.275550842e-01f,8.819212914e-01f,-4.713967443e-01f,8.577286005e-01f,-5.141027570e-01f,
8.314695954e-01f,-5.555702448e-01f,8.032075167e-01f,-5.956993103e-01f,7.730104327e-01f,-6.343932748e-01f,7.409511209e-01f,-6.715589762e-01f,
7.071067691e-01f,-7.071067691e-01f,6.715589762e-01f,-7.409511209e-01f,6.343932748e-01f,-7.730104327e-01f,5.956993103e-01f,-8.032075167e-01f,
5.555702448e-01f,-8.314695954e-01f,5.141027570e-01f,-8.577286005e-01f,4.713967443e-01f,-8.819212914e-01f,4.275550842e-01f,-9.039893150e-01f,
3.826834261e-01f,-9.238795042e-01f,3.368898630e-01f,-9.415440559e-01f,2.902846634e-01f,-9.569403529e-01f,2.429801822e-01f,-9.700312614e-01f,
1.950903237e-01f,-9.807852507e-01f,1.467304677e-01f,-9.891765118e-01f,9.801714122e-02f,-9.951847196e-01f,4.906767607e-02f,-9.987954497e-01f,
0.000000000e+00f,-1.000000000e+00f,-4.906767607e-02f,-9.987954497e-01f,-9.801714122e-02f,-9.951847196e-01f,-1.467304677e-01f,-9.891765118e-01f,
-1.950903237e-01f,-9.807852507e-01f,-2.429801822e-01f,-9.700312614e-01f,-2.902846634e-01f,-9.569403529e-01f,-3.368898630e-01f,-9.415440559e-01f,
1.000000000e+00f,0.000000000e+00f,9.982656240e-01f,-5.887080356e-02f,9.930684566e-01f,-1.175373942e-01f,9.844265580e-01f,-1.757962853e-01f,
9.723699093e-01f,-2.334453613e-01f,9.569403529e-01f,-2.902846634e-01f,9.381913543e-01f,-3.461170495e-01f,9.161879420e-01f,-4.007488191e-01f,
8.910065293e-01f,-4.539904892e-01f,8.627343774e-01f,-5.056573749e-01f,8.314695954e-01f,-5.555702448e-01f,7.973206639e-01f,-6.035559177e-01f,
7.604059577e-01f,-6.494480371e-01f,7.208535671e-01f,-6.930873394e-01f,6.788007617e-01f,-7.343224883e-01f,6.343932748e-01f,-7.730104327e-01f,
5.877852440e-01f,-8.090170026e-01f,5.391383171e-01f,-8.422172070e-01f,4.886212349e-01f,-8.724960089e-01f,4.364092350e-01f,-8.997482657e-01f,
3.826834261e-01f,-9.238795042e-01f,3.276301920e-01f,-9.448060393e-01f,2.714404464e-01f,-9.624552131e-01f,2.143091559e-01f,-9.767658710e-01f,
1.564344615e-01f,-9.876883626e-01f,9.801714122e-02f,-9.951847196e-01f,3.925981745e-02f,-9.992290139e-01f,-1.963369176e-02f,-9.998072386e-01f,
-7.845909894e-02f,-9.969173074e-01f,-1.370123476e-01f,-9.905693531e-01f,-1.950903237e-01f,-9.807852507e-01f,-2.524915636e-01f,-9.675990939e-01f,
-3.090170026e-01f,-9.510565400e-01f,-3.644705117e-01f,-9.312149286e-01f,-4.186597466e-01f,-9.081431627e-01f,-4.713967443e-01f,-8.819212914e-01f,
-5.224985480e-01f,-8.526401520e-01f,-5.717879534e-01f,-8.204014301e-01f,-6.190939546e-01f,-7.853169441e-01f,-6.642524600e-01f,-7.475083470e-01f,
1.000000000e+00f,0.000000000e+00f,9.976395369e-01f,-6.866826117e-02f,9.905693531e-01f,-1.370123476e-01f,9.788227677e-01f,-2.047096044e-01f,
9.624552131e-01f,-2.714404464e-01f,9.415440559e-01f,-3.368898630e-01f,9.161879420e-01f,-4.007488191e-01f,8.865066171e-01f,-4.627159238e-01f,
8.526401520e-01f,-5.224985480e-01f,8.147484660e-01f,-5.798145533e-01f,7.730104327e-01f,-6.343932748e-01f,7.276231050e-01f,-6.859771013e-01f,
6.788007617e-01f,-7.343224883e-01f,6.267738342e-01f,-7.792012691e-01f,5.717879534e-01f,-8.204014301e-01f,5.141027570e-01f,-8.577286005e-01f,
4.539904892e-01f,-8.910065293e-01f,3.917350173e-01f,-9.200780988e-01f,3.276301920e-01f,-9.448060393e-01f,2.619786263e-01f,-9.650736451e-01f,
1.950903237e-01f,-9.807852507e-01f,1.272810102e-01f,-9.918667078e-01f,5.887080356e-02f,-9.982656240e-01f,-9.817318991e-03f,-9.999517798e-01f,
-7.845909894e-02f,-9.969173074e-01f,-1.467304677e-01f,-9.891765118e-01f,-2.143091559e-01f,-9.767658710e-01f,-2.808761001e-01f,-9.597440362e-01f,
-3.461170495e-01f,-9.381913543e-01f,-4.097240269e-01f,-9.122095108e-01f,-4.713967443e-01f,-8.819212914e-01f,-5.308440328e-01f,-8.474695683e-01f,
-5.877852440e-01f,-8.090170026e-01f,-6.419516206e-01f,-7.667451501e-01f,-6.930873394e-01f,-7.208535671e-01f,-7.409511209e-01f,-6.715589762e-01f,
-7.853169441e-01f,-6.190939546e-01f,-8.259753585e-01f,-5.637062788e-01f,-8.627343774e-01f,-5.056573749e-01f,-8.954205513e-01f,-4.452213347e-01f,
1.000000000e+00f,0.000000000e+00f,9.969173074e-01f,-7.845909894e-02f,9.876883626e-01f,-1.564344615e-01f,9.723699093e-01f,-2.334453613e-01f,
9.510565400e-01f,-3.090170026e-01f,9.238795042e-01f,-3.826834261e-01f,8.910065293e-01f,-4.539904892e-01f,8.526401520e-01f,-5.224985480e-01f,
8.090170026e-01f,-5.877852440e-01f,7.604059577e-01f,-6.494480371e-01f,7.071067691e-01f,-7.071067691e-01f,6.494480371e-01f,-7.604059577e-01f,
5.877852440e-01f,-8.090170026e-01f,5.224985480e-01f,-8.526401520e-01f,4.539904892e-01f,-8.910065293e-01f,3.826834261e-01f,-9.238795042e-01f,
3.090170026e-01f,-9.510565400e-01f,2.334453613e-01f,-9.723699093e-01f,1.564344615e-01f,-9.876883626e-01f,7.845909894e-02f,-9.969173074e-01f,
0.000000000e+00f,-1.000000000e+00f,-7.845909894e-02f,-9.969173074e-01f,-1.564344615e-01f,-9.876883626e-01f,-2.334453613e-01f,-9.723699093e-01f,
-3.090170026e-01f,-9.510565400e-01f,-3.826834261e-01f,-9.238795042e-01f,-4.539904892e-01f,-8.910065293e-01f,-5.224985480e-01f,-8.526401520e-01f,
-5.877852440e-01f,-8.090170026e-01f,-6.494480371e-01f,-7.604059577e-01f,-7.071067691e-01f,-7.071067691e-01f,-7.604059577e-01f,-6.494480371e-01f,
-8.090170026e-01f,-5.877852440e-01f,-8.526401520e-01f,-5.224985480e-01f,-8.910065293e-01f,-4.539904892e-01f,-9.238795042e-01f,-3.826834261e-01f,
-9.510565400e-01f,-3.090170026e-01f,-9.723699093e-01f,-2.334453613e-01f,-9.876883626e-01f,-1.564344615e-01f,-9.969173074e-01f,-7.845909894e-02f,
1.000000000e+00f,0.000000000e+00f,9.960990548e-01f,-8.824236691e-02f,9.844265580e-01f,-1.757962853e-01f,9.650736451e-01f,-2.619786263e-01f,
9.381913543e-01f,-3.461170495e-01f,9.039893150e-01f,-4.275550842e-01f,8.627343774e-01f,-5.056573749e-01f,8.147484660e-01f,-5.798145533e-01f,
7.604059577e-01f,-6.494480371e-01f,7.001308203e-01f,-7.140145898e-01f,6.343932748e-01f,-7.730104327e-01f,5.637062788e-01f,-8.259753585e-01f,
4.886212349e-01f,-8.724960089e-01f,4.097240269e-01f,-9.122095108e-01f,3.276301920e-01f,-9.448060393e-01f,2.429801822e-01f,-9.700312614e-01f,
1.564344615e-01f,-9.876883626e-01f,6.866826117e-02f,-9.976395369e-01f,-1.963369176e-02f,-9.998072386e-01f,-1.077824607e-01f,-9.941744804e-01f,
-1.950903237e-01f,-9.807852507e-01f,-2.808761001e-01f,-9.597440362e-01f,-3.644705117e-01f,-9.312149286e-01f,-4.452213347e-01f,-8.954205513e-01f,
-5.224985480e-01f,-8.526401520e-01f,-5.956993103e-01f,-8.032075167e-01f,-6.642524600e-01f,-7.475083470e-01f,-7.276231050e-01f,-6.859771013e-01f,
-7.853169441e-01f,-6.190939546e-01f,-8.368837237e-01f,-5.473806858e-01f,-8.819212914e-01f,-4.713967443e-01f,-9.200780988e-01f,-3.917350173e-01f,
-9.510565400e-01f,-3.090170026e-01f,-9.746148586e-01f,-2.238880545e-01f,-9.905693531e-01f,-1.370123476e-01f,-9.987954497e-01f,-4.906767607e-02f,
-9.992290139e-01f,3.925981745e-02f,-9.918667078e-01f,1.272810102e-01f,-9.767658710e-01f,2.143091559e-01f,-9.540444016e-01f,2.996652722e-01f,
1.000000000e+00f,0.000000000e+00f,9.951847196e-01f,-9.801714122e-02f,9.807852507e-01f,-1.950903237e-01f,9.569403529e-01f,-2.902846634e-01f,
9.238795042e-01f,-3.826834261e-01f,8.819212914e-01f,-4.713967443e-01f,8.314695954e-01f,-5.555702448e-01f,7.730104327e-01f,-6.343932748e-01f,
7.071067691e-01f,-7.071067691e-01f,6.343932748e-01f,-7.730104327e-01f,5.555702448e-01f,-8.314695954e-01f,4.713967443e-01f,-8.819212914e-01f,
3.826834261e-01f,-9.238795042e-01f,2.902846634e-01f,-9.569403529e-01f,1.950903237e-01f,-9.807852507e-01f,9.801714122e-02f,-9.951847196e-01f,
0.000000000e+00f,-1.000000000e+00f,-9.801714122e-02f,-9.951847196e-01f,-1.950903237e-01f,-9.807852507e-01f,-2.902846634e-01f,-9.569403529e-01f,
-3.826834261e-01f,-9.238795042e-01f,-4.713967443e-01f,-8.819212914e-01f,-5.555702448e-01f,-8.314695954e-01f,-6.343932748e-01f,-7.730104327e-01f,
-7.071067691e-01f,-7.071067691e-01f,-7.730104327e-01f,-6.343932748e-01f,-8.314695954e-01f,-5.555702448e-01f,-8.819212914e-01f,-4.713967443e-01f,
-9.238795042e-01f,-3.826834261e-01f,-9.569403529e-01f,-2.902846634e-01f,-9.807852507e-01f,-1.950903237e-01f,-9.951847196e-01f,-9.801714122e-02f,
-1.000000000e+00f,0.000000000e+00f,-9.951847196e-01f,9.801714122e-02f,-9.807852507e-01f,1.950903237e-01f,-9.569403529e-01f,2.902846634e-01f,
-9.238795042e-01f,3.826834261e-01f,-8.819212914e-01f,4.713967443e-01f,-8.314695954e-01f,5.555702448e-01f,-7.730104327e-01f,6.343932748e-01f,
1.000000000e+00f,0.000000000e+00f,9.941744804e-01f,-1.077824607e-01f,9.767658710e-01f,-2.143091559e-01f,9.479769468e-01f,-3.183389306e-01f,
9.081431627e-01f,-4.186597466e-01f,8.577286005e-01f,-5.141027570e-01f,7.973206639e-01f,-6.035559177e-01f,7.276231050e-01f,-6.859771013e-01f,
6.494480371e-01f,-7.604059577e-01f,5.637062788e-01f,-8.259753585e-01f,4.713967443e-01f,-8.819212914e-01f,3.735949695e-01f,-9.275919199e-01f,
2.714404464e-01f,-9.624552131e-01f,1.661233753e-01f,-9.861049652e-01f,5.887080356e-02f,-9.982656240e-01f,-4.906767607e-02f,-9.987954497e-01f,
-1.564344615e-01f,-9.876883626e-01f,-2.619786263e-01f,-9.650736451e-01f,-3.644705117e-01f,-9.312149286e-01f,-4.627159238e-01f,-8.865066171e-01f,
-5.555702448e-01f,-8.314695954e-01f,-6.419516206e-01f,-7.667451501e-01f,-7.208535671e-01f,-6.930873394e-01f,-7.913569212e-01f,-6.113544106e-01f,
-8.526401520e-01f,-5.224985480e-01f,-9.039893150e-01f,-4.275550842e-01f,-9.448060393e-01f,-3.276301920e-01f,-9.746148586e-01f,-2.238880545e-01f,
-9.930684566e-01f,-1.175373942e-01f,-9.999517798e-01f,-9.817318991e-03f,-9.951847196e-01f,9.801714122e-02f,-9.788227677e-01f,2.047096044e-01f,
-9.510565400e-01f,3.090170026e-01f,-9.122095108e-01f,4.097240269e-01f,-8.627343774e-01f,5.056573749e-01f,-8.032075167e-01f,5.956993103e-01f,
-7.343224883e-01f,6.788007617e-01f,-6.568819284e-01f,7.539935112e-01f,-5.717879534e-01f,8.204014301e-01f,-4.800321162e-01f,8.772509098e-01f,
1.000000000e+00f,0.000000000e+00f,9.930684566e-01f,-1.175373942e-01f,9.723699093e-01f,-2.334453613e-01f,9.381913543e-01f,-3.461170495e-01f,
8.910065293e-01f,-4.539904892e-01f,8.314695954e-01f,-5.555702448e-01f,7.604059577e-01f,-6.494480371e-01f,6.788007617e-01f,-7.343224883e-01f,
5.877852440e-01f,-8.090170026e-01f,4.886212349e-01f,-8.724960089e-01f,3.826834261e-01f,-9.238795042e-01f,2.714404464e-01f,-9.624552131e-01f,
1.564344615e-01f,-9.876883626e-01f,3.925981745e-02f,-9.992290139e-01f,-7.845909894e-02f,-9.969173074e-01f,-1.950903237e-01f,-9.807852507e-01f,
-3.090170026e-01f,-9.510565400e-01f,-4.186597466e-01f,-9.081431627e-01f,-5.224985480e-01f,-8.526401520e-01f,-6.190939546e-01f,-7.853169441e-01f,
-7.071067691e-01f,-7.071067691e-01f,-7.853169441e-01f,-6.190939546e-01f,-8.526401520e-01f,-5.224985480e-01f,-9.081431627e-01f,-4.186597466e-01f,
-9.510565400e-01f,-3.090170026e-01f,-9.807852507e-01f,-1.950903237e-01f,-9.969173074e-01f,-7.845909894e-02f,-9.992290139e-01f,3.925981745e-02f,
-9.876883626e-01f,1.564344615e-01f,-9.624552131e-01f,2.714404464e-01f,-9.238795042e-01f,3.826834261e-01f,-8.724960089e-01f,4.886212349e-01f,
-8.090170026e-01f,5.877852440e-01f,-7.343224883e-01f,6.788007617e-01f,-6.494480371e-01f,7.604059577e-01f,-5.555702448e-01f,8.314695954e-01f,
-4.539904892e-01f,8.910065293e-01f,-3.461170495e-01f,9.381913543e-01f,-2.334453613e-01f,9.723699093e-01f,-1.175373942e-01f,9.930684566e-01f,
1.000000000e+00f,0.000000000e+00f,9.918667078e-01f,-1.272810102e-01f,9.675990939e-01f,-2.524915636e-01f,9.275919199e-01f,-3.735949695e-01f,
8.724960089e-01f,-4.886212349e-01f,8.032075167e-01f,-5.956993103e-01f,7.208535671e-01f,-6.930873394e-01f,6.267738342e-01f,-7.792012691e-01f,
5.224985480e-01f,-8.526401520e-01f,4.097240269e-01f,-9.122095108e-01f,2.902846634e-01f,-9.569403529e-01f,1.661233753e-01f,-9.861049652e-01f,
3.925981745e-02f,-9.992290139e-01f,-8.824236691e-02f,-9.960990548e-01f,-2.143091559e-01f,-9.767658710e-01f,-3.368898630e-01f,-9.415440559e-01f,
-4.539904892e-01f,-8.910065293e-01f,-5.637062788e-01f,-8.259753585e-01f,-6.642524600e-01f,-7.475083470e-01f,-7.539935112e-01f,-6.568819284e-01f,
-8.314695954e-01f,-5.555702448e-01f,-8.954205513e-01f,-4.452213347e-01f,-9.448060393e-01f,-3.276301920e-01f,-9.788227677e-01f,-2.047096044e-01f,
-9.969173074e-01f,-7.845909894e-02f,-9.987954497e-01f,4.906767607e-02f,-9.844265580e-01f,1.757962853e-01f,-9.540444016e-01f,2.996652722e-01f,
-9.081431627e-01f,4.186597466e-01f,-8.474695683e-01f,5.308440328e-01f,-7.730104327e-01f,6.343932748e-01f,-6.859771013e-01f,7.276231050e-01f,
-5.877852440e-01f,8.090170026e-01f,-4.800321162e-01f,8.772509098e-01f,-3.644705117e-01f,9.312149286e-01f,-2.429801822e-01f,9.700312614e-01f,
-1.175373942e-01f,9.930684566e-01f,9.817318991e-03f,9.999517798e-01f,1.370123476e-01f,9.905693531e-01f,2.619786263e-01f,9.650736451e-01f,
1.000000000e+00f,0.000000000e+00f,9.905693531e-01f,-1.370123476e-01f,9.624552131e-01f,-2.714404464e-01f,9.161879420e-01f,-4.007488191e-01f,
8.526401520e-01f,-5.224985480e-01f,7.730104327e-01f,-6.343932748e-01f,6.788007617e-01f,-7.343224883e-01f,5.717879534e-01f,-8.204014301e-01f,
4.539904892e-01f,-8.910065293e-01f,3.276301920e-01f,-9.448060393e-01f,1.950903237e-01f,-9.807852507e-01f,5.887080356e-02f,-9.982656240e-01f,
-7.845909894e-02f,-9.969173074e-01f,-2.143091559e-01f,-9.767658710e-01f,-3.461170495e-01f,-9.381913543e-01f,-4.713967443e-01f,-8.819212914e-01f,
-5.877852440e-01f,-8.090170026e-01f,-6.930873394e-01f,-7.208535671e-01f,-7.853169441e-01f,-6.190939546e-01f,-8.627343774e-01f,-5.056573749e-01f,
-9.238795042e-01f,-3.826834261e-01f,-9.675990939e-01f,-2.524915636e-01f,-9.930684566e-01f,-1.175373942e-01f,-9.998072386e-01f,1.963369176e-02f,
-9.876883626e-01f,1.564344615e-01f,-9.569403529e-01f,2.902846634e-01f,-9.081431627e-01f,4.186597466e-01f,-8.422172070e-01f,5.391383171e-01f,
-7.604059577e-01f,6.494480371e-01f,-6.642524600e-01f,7.475083470e-01f,-5.555702448e-01f,8.314695954e-01f,-4.364092350e-01f,8.997482657e-01f,
-3.090170026e-01f,9.510565400e-01f,-1.757962853e-01f,9.844265580e-01f,-3.925981745e-02f,9.992290139e-01f,9.801714122e-02f,9.951847196e-01f,
2.334453613e-01f,9.723699093e-01f,3.644705117e-01f,9.312149286e-01f,4.886212349e-01f,8.724960089e-01f,6.035559177e-01f,7.973206639e-01f,
1.000000000e+00f,0.000000000e+00f,9.891765118e-01f,-1.467304677e-01f,9.569403529e-01f,-2.902846634e-01f,9.039893150e-01f,-4.275550842e-01f,
8.314695954e-01f,-5.555702448e-01f,7.409511209e-01f,-6.715589762e-01f,6.343932748e-01f,-7.730104327e-01f,5.141027570e-01f,-8.577286005e-01f,
3.826834261e-01f,-9.238795042e-01f,2.429801822e-01f,-9.700312614e-01f,9.801714122e-02f,-9.951847196e-01f,-4.906767607e-02f,-9.987954497e-01f,
-1.950903237e-01f,-9.807852507e-01f,-3.368898630e-01f,-9.415440559e-01f,-4.713967443e-01f,-8.819212914e-01f,-5.956993103e-01f,-8.032075167e-01f,
-7.071067691e-01f,-7.071067691e-01f,-8.032075167e-01f,-5.956993103e-01f,-8.819212914e-01f,-4.713967443e-01f,-9.415440559e-01f,-3.368898630e-01f,
-9.807852507e-01f,-1.950903237e-01f,-9.987954497e-01f,-4.906767607e-02f,-9.951847196e-01f,9.801714122e-02f,-9.700312614e-01f,2.429801822e-01f,
-9.238795042e-01f,3.826834261e-01f,-8.577286005e-01f,5.141027570e-01f,-7.730104327e-01f,6.343932748e-01f,-6.715589762e-01f,7.409511209e-01f,
-5.555702448e-01f,8.314695954e-01f,-4.275550842e-01f,9.039893150e-01f,-2.902846634e-01f,9.569403529e-01f,-1.467304677e-01f,9.891765118e-01f,
0.000000000e+00f,1.000000000e+00f,1.467304677e-01f,9.891765118e-01f,2.902846634e-01f,9.569403529e-01f,4.275550842e-01f,9.039893150e-01f,
5.555702448e-01f,8.314695954e-01f,6.715589762e-01f,7.409511209e-01f,7.730104327e-01f,6.343932748e-01f,8.577286005e-01f,5.141027570e-01f,
1.000000000e+00f,0.000000000e+00f,9.876883626e-01f,-1.564344615e-01f,9.510565400e-01f,-3.090170026e-01f,8.910065293e-01f,-4.539904892e-01f,
1.000000000e+00f,0.000000000e+00f,9.510565400e-01f,-3.090170026e-01f,8.090170026e-01f,-5.877852440e-01f,5.877852440e-01f,-8.090170026e-01f,
1.000000000e+00f,0.000000000e+00f,8.910065293e-01f,-4.539904892e-01f,5.877852440e-01f,-8.090170026e-01f,1.564344615e-01f,-9.876883626e-01f,
1.000000000e+00f,0.000000000e+00f,8.090170026e-01f,-5.877852440e-01f,3.090170026e-01f,-9.510565400e-01f,-3.090170026e-01f,-9.510565400e-01f,
1.000000000e+00f,0.000000000e+00f,7.071067691e-01f,-7.071067691e-01f,0.000000000e+00f,-1.000000000e+00f,-7.071067691e-01f,-7.071067691e-01f,
1.000000000e+00f,0.000000000e+00f,5.877852440e-01f,-8.090170026e-01f,-3.090170026e-01f,-9.510565400e-01f,-9.510565400e-01f,-3.090170026e-01f,
1.000000000e+00f,0.000000000e+00f,4.539904892e-01f,-8.910065293e-01f,-5.877852440e-01f,-8.090170026e-01f,-9.876883626e-01f,1.564344615e-01f,
1.000000000e+00f,0.000000000e+00f,3.090170026e-01f,-9.510565400e-01f,-8.090170026e-01f,-5.877852440e-01f,-8.090170026e-01f,5.877852440e-01f,
1.000000000e+00f,0.000000000e+00f,1.564344615e-01f,-9.876883626e-01f,-9.510565400e-01f,-3.090170026e-01f,-4.539904892e-01f,8.910065293e-01f,
0.000000000e+00f,0.000000000e+00f,0.000000000e+00f,0.000000000e+00f,0.000000000e+00f,0.000000000e+00f,0.000000000e+00f,0.000000000e+00f,
1.000000000e+00f,0.000000000e+00f,9.996988177e-01f,-2.454122901e-02f,9.987954497e-01f,-4.906767607e-02f,9.972904325e-01f,-7.356456667e-02f,
9.951847196e-01f,-9.801714122e-02f,9.924795628e-01f,-1.224106774e-01f,9.891765118e-01f,-1.467304677e-01f,9.852776527e-01f,-1.709618866e-01f,
9.807852507e-01f,-1.950903237e-01f,9.757021070e-01f,-2.191012353e-01f,9.700312614e-01f,-2.429801822e-01f,9.637760520e-01f,-2.667127550e-01f,
9.569403529e-01f,-2.902846634e-01f,9.495281577e-01f,-3.136817515e-01f,9.415440559e-01f,-3.368898630e-01f,9.329928160e-01f,-3.598950505e-01f,
1.000000000e+00f,0.000000000e+00f,9.987954497e-01f,-4.906767607e-02f,9.951847196e-01f,-9.801714122e-02f,9.891765118e-01f,-1.467304677e-01f,
9.807852507e-01f,-1.950903237e-01f,9.700312614e-01f,-2.429801822e-01f,9.569403529e-01f,-2.902846634e-01f,9.415440559e-01f,-3.368898630e-01f,
9.238795042e-01f,-3.826834261e-01f,9.039893150e-01f,-4.275550842e-01f,8.819212914e-01f,-4.713967443e-01f,8.577286005e-01f,-5.141027570e-01f,
8.314695954e-01f,-5.555702448e-01f,8.032075167e-01f,-5.956993103e-01f,7.730104327e-01f,-6.343932748e-01f,7.409511209e-01f,-6.715589762e-01f,
1.000000000e+00f,0.000000000e+00f,9.972904325e-01f,-7.356456667e-02f,9.891765118e-01f,-1.467304677e-01f,9.757021070e-01f,-2.191012353e-01f,
9.569403529e-01f,-2.902846634e-01f,9.329928160e-01f,-3.598950505e-01f,9.039893150e-01f,-4.275550842e-01f,8.700869679e-01f,-4.928981960e-01f,
8.314695954e-01f,-5.555702448e-01f,7.883464098e-01f,-6.152315736e-01f,7.409511209e-01f,-6.715589762e-01f,6.895405650e-01f,-7.242470980e-01f,
6.343932748e-01f,-7.730104327e-01f,5.758081675e-01f,-8.175848126e-01f,5.141027570e-01f,-8.577286005e-01f,4.496113360e-01f,-8.932242990e-01f,
1.000000000e+00f,0.000000000e+00f,9.951847196e-01f,-9.801714122e-02f,9.807852507e-01f,-1.950903237e-01f,9.569403529e-01f,-2.902846634e-01f,
9.238795042e-01f,-3.826834261e-01f,8.819212914e-01f,-4.713967443e-01f,8.314695954e-01f,-5.555702448e-01f,7.730104327e-01f,-6.343932748e-01f,
7.071067691e-01f,-7.071067691e-01f,6.343932748e-01f,-7.730104327e-01f,5.555702448e-01f,-8.314695954e-01f,4.713967443e-01f,-8.819212914e-01f,
3.826834261e-01f,-9.238795042e-01f,2.902846634e-01f,-9.569403529e-01f,1.950903237e-01f,-9.807852507e-01f,9.801714122e-02f,-9.951847196e-01f,
1.000000000e+00f,0.000000000e+00f,9.924795628e-01f,-1.224106774e-01f,9.700312614e-01f,-2.429801822e-01f,9.329928160e-01f,-3.598950505e-01f,
8.819212914e-01f,-4.713967443e-01f,8.175848126e-01f,-5.758081675e-01f,7.409511209e-01f,-6.715589762e-01f,6.531728506e-01f,-7.572088242e-01f,
5.555702448e-01f,-8.314695954e-01f,4.496113360e-01f,-8.932242990e-01f,3.368898630e-01f,-9.415440559e-01f,2.191012353e-01f,-9.757021070e-01f,
9.801714122e-02f,-9.951847196e-01f,-2.454122901e-02f,-9.996988177e-01f,-1.467304677e-01f,-9.891765118e-01f,-2.667127550e-01f,-9.637760520e-01f,
1.000000000e+00f,0.000000000e+00f,9.891765118e-01f,-1.467304677e-01f,9.569403529e-01f,-2.902846634e-01f,9.039893150e-01f,-4.275550842e-01f,
8.314695954e-01f,-5.555702448e-01f,7.409511209e-01f,-6.715589762e-01f,6.343932748e-01f,-7.730104327e-01f,5.141027570e-01f,-8.577286005e-01f,
3.826834261e-01f,-9.238795042e-01f,2.429801822e-01f,-9.700312614e-01f,9.801714122e-02f,-9.951847196e-01f,-4.906767607e-02f,-9.987954497e-01f,
-1.950903237e-01f,-9.807852507e-01f,-3.368898630e-01f,-9.415440559e-01f,-4.713967443e-01f,-8.819212914e-01f,-5.956993103e-01f,-8.032075167e-01f,
1.000000000e+00f,0.000000000e+00f,9.852776527e-01f,-1.709618866e-01f,9.415440559e-01f,-3.368898630e-01f,8.700869679e-01f,-4.928981960e-01f,
7.730104327e-01f,-6.343932748e-01f,6.531728506e-01f,-7.572088242e-01f,5.141027570e-01f,-8.577286005e-01f,3.598950505e-01f,-9.329928160e-01f,
1.950903237e-01f,-9.807852507e-01f,2.454122901e-02f,-9.996988177e-01f,-1.467304677e-01f,-9.891765118e-01f,-3.136817515e-01f,-9.495281577e-01f,
-4.713967443e-01f,-8.819212914e-01f,-6.152315736e-01f,-7.883464098e-01f,-7.409511209e-01f,-6.715589762e-01f,-8.448535800e-01f,-5.349976420e-01f,
1.000000000e+00f,0.000000000e+00f,9.807852507e-01f,-1.950903237e-01f,9.238795042e-01f,-3.826834261e-01f,8.314695954e-01f,-5.555702448e-01f,
7.071067691e-01f,-7.071067691e-01f,5.555702448e-01f,-8.314695954e-01f,3.826834261e-01f,-9.238795042e-01f,1.950903237e-01f,-9.807852507e-01f,
0.000000000e+00f,-1.000000000e+00f,-1.950903237e-01f,-9.807852507e-01f,-3.826834261e-01f,-9.238795042e-01f,-5.555702448e-01f,-8.314695954e-01f,
-7.071067691e-01f,-7.071067691e-01f,-8.314695954e-01f,-5.555702448e-01f,-9.238795042e-01f,-3.826834261e-01f,-9.807852507e-01f,-1.950903237e-01f,
1.000000000e+00f,0.000000000e+00f,9.757021070e-01f,-2.191012353e-01f,9.039893150e-01f,-4.275550842e-01f,7.883464098e-01f,-6.152315736e-01f,
6.343932748e-01f,-7.730104327e-01f,4.496113360e-01f,-8.932242990e-01f,2.429801822e-01f,-9.700312614e-01f,2.454122901e-02f,-9.996988177e-01f,
-1.950903237e-01f,-9.807852507e-01f,-4.052413106e-01f,-9.142097831e-01f,-5.956993103e-01f,-8.032075167e-01f,-7.572088242e-01f,-6.531728506e-01f,
-8.819212914e-01f,-4.713967443e-01f,-9.637760520e-01f,-2.667127550e-01f,-9.987954497e-01f,-4.906767607e-02f,-9.852776527e-01f,1.709618866e-01f,
1.000000000e+00f,0.000000000e+00f,9.700312614e-01f,-2.429801822e-01f,8.819212914e-01f,-4.713967443e-01f,7.409511209e-01f,-6.715589762e-01f,
5.555702448e-01f,-8.314695954e-01f,3.368898630e-01f,-9.415440559e-01f,9.801714122e-02f,-9.951847196e-01f,-1.467304677e-01f,-9.891765118e-01f,
-3.826834261e-01f,-9.238795042e-01f,-5.956993103e-01f,-8.032075167e-01f,-7.730104327e-01f,-6.343932748e-01f,-9.039893150e-01f,-4.275550842e-01f,
-9.807852507e-01f,-1.950903237e-01f,-9.987954497e-01f,4.906767607e-02f,-9.569403529e-01f,2.902846634e-01f,-8.577286005e-01f,5.141027570e-01f,
1.000000000e+00f,0.000000000e+00f,9.637760520e-01f,-2.667127550e-01f,8.577286005e-01f,-5.141027570e-01f,6.895405650e-01f,-7.242470980e-01f,
4.713967443e-01f,-8.819212914e-01f,2.191012353e-01f,-9.757021070e-01f,-4.906767607e-02f,-9.987954497e-01f,-3.136817515e-01f,-9.495281577e-01f,
-5.555702448e-01f,-8.314695954e-01f,-7.572088242e-01f,-6.531728506e-01f,-9.039893150e-01f,-4.275550842e-01f,-9.852776527e-01f,-1.709618866e-01f,
-9.951847196e-01f,9.801714122e-02f,-9.329928160e-01f,3.598950505e-01f,-8.032075167e-01f,5.956993103e-01f,-6.152315736e-01f,7.883464098e-01f,
1.000000000e+00f,0.000000000e+00f,9.569403529e-01f,-2.902846634e-01f,8.314695954e-01f,-5.555702448e-01f,6.343932748e-01f,-7.730104327e-01f,
3.826834261e-01f,-9.238795042e-01f,9.801714122e-02f,-9.951847196e-01f,-1.950903237e-01f,-9.807852507e-01f,-4.713967443e-01f,-8.819212914e-01f,
-7.071067691e-01f,-7.071067691e-01f,-8.819212914e-01f,-4.713967443e-01f,-9.807852507e-01f,-1.950903237e-01f,-9.951847196e-01f,9.801714122e-02f,
-9.238795042e-01f,3.826834261e-01f,-7.730104327e-01f,6.343932748e-01f,-5.555702448e-01f,8.314695954e-01f,-2.902846634e-01f,9.569403529e-01f,
1.000000000e+00f,0.000000000e+00f,9.495281577e-01f,-3.136817515e-01f,8.032075167e-01f,-5.956993103e-01f,5.758081675e-01f,-8.175848126e-01f,
2.902846634e-01f,-9.569403529e-01f,-2.454122901e-02f,-9.996988177e-01f,-3.368898630e-01f,-9.415440559e-01f,-6.152315736e-01f,-7.883464098e-01f,
-8.314695954e-01f,-5.555702448e-01f,-9.637760520e-01f,-2.667127550e-01f,-9.987954497e-01f,4.906767607e-02f,-9.329928160e-01f,3.598950505e-01f,
-7.730104327e-01f,6.343932748e-01f,-5.349976420e-01f,8.448535800e-01f,-2.429801822e-01f,9.700312614e-01f,7.356456667e-02f,9.972904325e-01f,
1.000000000e+00f,0.000000000e+00f,9.415440559e-01f,-3.368898630e-01f,7.730104327e-01f,-6.343932748e-01f,5.141027570e-01f,-8.577286005e-01f,
1.950903237e-01f,-9.807852507e-01f,-1.467304677e-01f,-9.891765118e-01f,-4.713967443e-01f,-8.819212914e-01f,-7.409511209e-01f,-6.715589762e-01f,
-9.238795042e-01f,-3.826834261e-01f,-9.987954497e-01f,-4.906767607e-02f,-9.569403529e-01f,2.902846634e-01f,-8.032075167e-01f,5.956993103e-01f,
-5.555702448e-01f,8.314695954e-01f,-2.429801822e-01f,9.700312614e-01f,9.801714122e-02f,9.951847196e-01f,4.275550842e-01f,9.039893150e-01f,
1.000000000e+00f,0.000000000e+00f,9.329928160e-01f,-3.598950505e-01f,7.409511209e-01f,-6.715589762e-01f,4.496113360e-01f,-8.932242990e-01f,
9.801714122e-02f,-9.951847196e-01f,-2.667127550e-01f,-9.637760520e-01f,-5.956993103e-01f,-8.032075167e-01f,-8.448535800e-01f,-5.349976420e-01f,
-9.807852507e-01f,-1.950903237e-01f,-9.852776527e-01f,1.709618866e-01f,-8.577286005e-01f,5.141027570e-01f,-6.152315736e-01f,7.883464098e-01f,
-2.902846634e-01f,9.569403529e-01f,7.356456667e-02f,9.972904325e-01f,4.275550842e-01f,9.039893150e-01f,7.242470980e-01f,6.895405650e-01f};
static __device__ const __attribute__((aligned(128))) float TC[8192] = {
5.000000000e-01f,5.000000000e-01f,5.000000000e-01f,-5.000000000e-01f,5.000000000e-01f,5.000000000e-01f,1.000000000e+00f,0.000000000e+00f,
1.000000000e+00f,0.000000000e+00f,7.071067691e-01f,-7.071067691e-01f,0.000000000e+00f,-1.000000000e+00f,-7.071067691e-01f,-7.071067691e-01f,
1.000000000e+00f,0.000000000e+00f,0.000000000e+00f,-1.000000000e+00f,-1.000000000e+00f,0.000000000e+00f,0.000000000e+00f,1.000000000e+00f,
1.000000000e+00f,0.000000000e+00f,-7.071067691e-01f,-7.071067691e-01f,0.000000000e+00f,1.000000000e+00f,7.071067691e-01f,-7.071067691e-01f,
5.000000000e-01f,5.000000000e-01f,5.000000000e-01f,-5.000000000e-01f,5.000000000e-01f,5.000000000e-01f,9.999247193e-01f,-1.227153838e-02f,
9.999952912e-01f,-3.067956772e-03f,7.049340606e-01f,-7.092728019e-01f,-3.067956772e-03f,-9.999952912e-01f,-7.092728019e-01f,-7.049340606e-01f,
9.999811649e-01f,-6.135884672e-03f,-6.135884672e-03f,-9.999811649e-01f,-9.999811649e-01f,6.135884672e-03f,6.135884672e-03f,9.999811649e-01f,
9.999576211e-01f,-9.203754365e-03f,-7.135848403e-01f,-7.005687952e-01f,9.203754365e-03f,9.999576211e-01f,7.005687952e-01f,-7.135848403e-01f,
5.000000000e-01f,5.000000000e-01f,5.000000000e-01f,-5.000000000e-01f,5.000000000e-01f,5.000000000e-01f,9.996988177e-01f,-2.454122901e-02f,
9.999811649e-01f,-6.135884672e-03f,7.027547359e-01f,-7.114322186e-01f,-6.135884672e-03f,-9.999811649e-01f,-7.114322186e-01f,-7.027547359e-01f,
9.999247193e-01f,-1.227153838e-02f,-1.227153838e-02f,-9.999247193e-01f,-9.999247193e-01f,1.227153838e-02f,1.227153838e-02f,9.999247193e-01f,
9.998306036e-01f,-1.840673015e-02f,-7.200025320e-01f,-6.939714551e-01f,1.840673015e-02f,9.998306036e-01f,6.939714551e-01f,-7.200025320e-01f,
5.000000000e-01f,5.000000000e-01f,5.000000000e-01f,-5.000000000e-01f,5.000000000e-01f,5.000000000e-01f,9.993223548e-01f,-3.680722415e-02f,
9.999576211e-01f,-9.203754365e-03f,7.005687952e-01f,-7.135848403e-01f,-9.203754365e-03f,-9.999576211e-01f,-7.135848403e-01f,-7.005687952e-01f,
9.998306036e-01f,-1.840673015e-02f,-1.840673015e-02f,-9.998306036e-01f,-9.998306036e-01f,1.840673015e-02f,1.840673015e-02f,9.998306036e-01f,
9.996188283e-01f,-2.760814503e-02f,-7.263591290e-01f,-6.873153448e-01f,2.760814503e-02f,9.996188283e-01f,6.873153448e-01f,-7.263591290e-01f,
5.000000000e-01f,5.000000000e-01f,5.000000000e-01f,-5.000000000e-01f,5.000000000e-01f,5.000000000e-01f,9.987954497e-01f,-4.906767607e-02f,
9.999247193e-01f,-1.227153838e-02f,6.983762383e-01f,-7.157308459e-01f,-1.227153838e-02f,-9.999247193e-01f,-7.157308459e-01f,-6.983762383e-01f,
9.996988177e-01f,-2.454122901e-02f,-2.454122901e-02f,-9.996988177e-01f,-9.996988177e-01f,2.454122901e-02f,2.454122901e-02f,9.996988177e-01f,
9.993223548e-01f,-3.680722415e-02f,-7.326542735e-01f,-6.806010008e-01f,3.680722415e-02f,9.993223548e-01f,6.806010008e-01f,-7.326542735e-01f,
5.000000000e-01f,5.000000000e-01f,5.000000000e-01f,-5.000000000e-01f,5.000000000e-01f,5.000000000e-01f,9.981181026e-01f,-6.132073700e-02f,
9.998823404e-01f,-1.533920597e-02f,6.961771250e-01f,-7.178700566e-01f,-1.533920597e-02f,-9.998823404e-01f,-7.178700566e-01f,-6.961771250e-01f,
9.995294213e-01f,-3.067480400e-02f,-3.067480400e-02f,-9.995294213e-01f,-9.995294213e-01f,3.067480400e-02f,3.067480400e-02f,9.995294213e-01f,
9.989413023e-01f,-4.600318149e-02f,-7.388873100e-01f,-6.738290191e-01f,4.600318149e-02f,9.989413023e-01f,6.738290191e-01f,-7.388873100e-01f,
5.000000000e-01f,5.000000000e-01f,5.000000000e-01f,-5.000000000e-01f,5.000000000e-01f,5.000000000e-01f,9.972904325e-01f,-7.356456667e-02f,
9.998306036e-01f,-1.840673015e-02f,6.939714551e-01f,-7.200025320e-01f,-1.840673015e-02f,-9.998306036e-01f,-7.200025320e-01f,-6.939714551e-01f,
9.993223548e-01f,-3.680722415e-02f,-3.680722415e-02f,-9.993223548e-01f,-9.993223548e-01f,3.680722415e-02f,3.680722415e-02f,9.993223548e-01f,
9.984755516e-01f,-5.519524589e-02f,-7.450577617e-01f,-6.669999361e-01f,5.519524589e-02f,9.984755516e-01f,6.669999361e-01f,-7.450577617e-01f,
5.000000000e-01f,5.000000000e-01f,5.000000000e-01f,-5.000000000e-01f,5.000000000e-01f,5.000000000e-01f,9.963126183e-01f,-8.579730988e-02f,
9.997693896e-01f,-2.147408016e-02f,6.917592287e-01f,-7.221282125e-01f,-2.147408016e-02f,-9.997693896e-01f,-7.221282125e-01f,-6.917592287e-01f,
9.990777373e-01f,-4.293825850e-02f,-4.293825850e-02f,-9.990777373e-01f,-9.990777373e-01f,4.293825850e-02f,4.293825850e-02f,9.990777373e-01f,
9.979252815e-01f,-6.438262761e-02f,-7.511651516e-01f,-6.601143479e-01f,6.438262761e-02f,9.979252815e-01f,6.601143479e-01f,-7.511651516e-01f,
5.000000000e-01f,5.000000000e-01f,5.000000000e-01f,-5.000000000e-01f,5.000000000e-01f,5.000000000e-01f,9.951847196e-01f,-9.801714122e-02f,
9.996988177e-01f,-2.454122901e-02f,6.895405650e-01f,-7.242470980e-01f,-2.454122901e-02f,-9.996988177e-01f,-7.242470980e-01f,-6.895405650e-01f,
9.987954497e-01f,-4.906767607e-02f,-4.906767607e-02f,-9.987954497e-01f,-9.987954497e-01f,4.906767607e-02f,4.906767607e-02f,9.987954497e-01f,
9.972904325e-01f,-7.356456667e-02f,-7.572088242e-01f,-6.531728506e-01f,7.356456667e-02f,9.972904325e-01f,6.531728506e-01f,-7.572088242e-01f,
5.000000000e-01f,5.000000000e-01f,5.000000000e-01f,-5.000000000e-01f,5.000000000e-01f,5.000000000e-01f,9.939069748e-01f,-1.102222055e-01f,
9.996188283e-01f,-2.760814503e-02f,6.873153448e-01f,-7.263591290e-01f,-2.760814503e-02f,-9.996188283e-01f,-7.263591290e-01f,-6.873153448e-01f,
9.984755516e-01f,-5.519524589e-02f,-5.519524589e-02f,-9.984755516e-01f,-9.984755516e-01f,5.519524589e-02f,5.519524589e-02f,9.984755516e-01f,
9.965711236e-01f,-8.274026215e-02f,-7.631884217e-01f,-6.461760402e-01f,8.274026215e-02f,9.965711236e-01f,6.461760402e-01f,-7.631884217e-01f,
5.000000000e-01f,5.000000000e-01f,5.000000000e-01f,-5.000000000e-01f,5.000000000e-01f,5.000000000e-01f,9.924795628e-01f,-1.224106774e-01f,
9.995294213e-01f,-3.067480400e-02f,6.850836873e-01f,-7.284643650e-01f,-3.067480400e-02f,-9.995294213e-01f,-7.284643650e-01f,-6.850836873e-01f,
9.981181026e-01f,-6.132073700e-02f,-6.132073700e-02f,-9.981181026e-01f,-9.981181026e-01f,6.132073700e-02f,6.132073700e-02f,9.981181026e-01f,
9.957674146e-01f,-9.190895408e-02f,-7.691033483e-01f,-6.391244531e-01f,9.190895408e-02f,9.957674146e-01f,6.391244531e-01f,-7.691033483e-01f,
5.000000000e-01f,5.000000000e-01f,5.000000000e-01f,-5.000000000e-01f,5.000000000e-01f,5.000000000e-01f,9.909026623e-01f,-1.345807016e-01f,
9.994305968e-01f,-3.374117240e-02f,6.828455329e-01f,-7.305627465e-01f,-3.374117240e-02f,-9.994305968e-01f,-7.305627465e-01f,-6.828455329e-01f,
9.977230430e-01f,-6.744392216e-02f,-6.744392216e-02f,-9.977230430e-01f,-9.977230430e-01f,6.744392216e-02f,6.744392216e-02f,9.977230430e-01f,
9.948793054e-01f,-1.010698602e-01f,-7.749531269e-01f,-6.320187449e-01f,1.010698602e-01f,9.948793054e-01f,6.320187449e-01f,-7.749531269e-01f,
5.000000000e-01f,5.000000000e-01f,5.000000000e-01f,-5.000000000e-01f,5.000000000e-01f,5.000000000e-01f,9.891765118e-01f,-1.467304677e-01f,
9.993223548e-01f,-3.680722415e-02f,6.806010008e-01f,-7.326542735e-01f,-3.680722415e-02f,-9.993223548e-01f,-7.326542735e-01f,-6.806010008e-01f,
9.972904325e-01f,-7.356456667e-02f,-7.356456667e-02f,-9.972904325e-01f,-9.972904325e-01f,7.356456667e-02f,7.356456667e-02f,9.972904325e-01f,
9.939069748e-01f,-1.102222055e-01f,-7.807372212e-01f,-6.248595119e-01f,1.102222055e-01f,9.939069748e-01f,6.248595119e-01f,-7.807372212e-01f,
5.000000000e-01f,5.000000000e-01f,5.000000000e-01f,-5.000000000e-01f,5.000000000e-01f,5.000000000e-01f,9.873014092e-01f,-1.588581502e-01f,
9.992047548e-01f,-3.987292573e-02f,6.783500314e-01f,-7.347388864e-01f,-3.987292573e-02f,-9.992047548e-01f,-7.347388864e-01f,-6.783500314e-01f,
9.968202710e-01f,-7.968243957e-02f,-7.968243957e-02f,-9.968202710e-01f,-9.968202710e-01f,7.968243957e-02f,7.968243957e-02f,9.968202710e-01f,
9.928504229e-01f,-1.193652153e-01f,-7.864552140e-01f,-6.176472902e-01f,1.193652153e-01f,9.928504229e-01f,6.176472902e-01f,-7.864552140e-01f,
5.000000000e-01f,5.000000000e-01f,5.000000000e-01f,-5.000000000e-01f,5.000000000e-01f,5.000000000e-01f,9.852776527e-01f,-1.709618866e-01f,
9.990777373e-01f,-4.293825850e-02f,6.760926843e-01f,-7.368165851e-01f,-4.293825850e-02f,-9.990777373e-01f,-7.368165851e-01f,-6.760926843e-01f,
9.963126183e-01f,-8.579730988e-02f,-8.579730988e-02f,-9.963126183e-01f,-9.963126183e-01f,8.579730988e-02f,8.579730988e-02f,9.963126183e-01f,
9.917097688e-01f,-1.284981072e-01f,-7.921065688e-01f,-6.103827953e-01f,1.284981072e-01f,9.917097688e-01f,6.103827953e-01f,-7.921065688e-01f,
5.000000000e-01f,5.000000000e-01f,5.000000000e-01f,-5.000000000e-01f,5.000000000e-01f,5.000000000e-01f,9.831054807e-01f,-1.830398887e-01f,
9.989413023e-01f,-4.600318149e-02f,6.738290191e-01f,-7.388873100e-01f,-4.600318149e-02f,-9.989413023e-01f,-7.388873100e-01f,-6.738290191e-01f,
9.957674146e-01f,-9.190895408e-02f,-9.190895408e-02f,-9.957674146e-01f,-9.957674146e-01f,9.190895408e-02f,9.190895408e-02f,9.957674146e-01f,
9.904850721e-01f,-1.376201212e-01f,-7.976908684e-01f,-6.030666232e-01f,1.376201212e-01f,9.904850721e-01f,6.030666232e-01f,-7.976908684e-01f,
5.000000000e-01f,5.000000000e-01f,5.000000000e-01f,-5.000000000e-01f,5.000000000e-01f,5.000000000e-01f,9.807852507e-01f,-1.950903237e-01f,
9.987954497e-01f,-4.906767607e-02f,6.715589762e-01f,-7.409511209e-01f,-4.906767607e-02f,-9.987954497e-01f,-7.409511209e-01f,-6.715589762e-01f,
9.951847196e-01f,-9.801714122e-02f,-9.801714122e-02f,-9.951847196e-01f,-9.951847196e-01f,9.801714122e-02f,9.801714122e-02f,9.951847196e-01f,
9.891765118e-01f,-1.467304677e-01f,-8.032075167e-01f,-5.956993103e-01f,1.467304677e-01f,9.891765118e-01f,5.956993103e-01f,-8.032075167e-01f,
5.000000000e-01f,5.000000000e-01f,5.000000000e-01f,-5.000000000e-01f,5.000000000e-01f,5.000000000e-01f,9.783173800e-01f,-2.071113735e-01f,
9.986402392e-01f,-5.213170499e-02f,6.692826152e-01f,-7.430079579e-01f,-5.213170499e-02f,-9.986402392e-01f,-7.430079579e-01f,-6.692826152e-01f,
9.945645928e-01f,-1.041216329e-01f,-1.041216329e-01f,-9.945645928e-01f,-9.945645928e-01f,1.041216329e-01f,1.041216329e-01f,9.945645928e-01f,
9.877841473e-01f,-1.558284014e-01f,-8.086561561e-01f,-5.882815719e-01f,1.558284014e-01f,9.877841473e-01f,5.882815719e-01f,-8.086561561e-01f,
5.000000000e-01f,5.000000000e-01f,5.000000000e-01f,-5.000000000e-01f,5.000000000e-01f,5.000000000e-01f,9.757021070e-01f,-2.191012353e-01f,
9.984755516e-01f,-5.519524589e-02f,6.669999361e-01f,-7.450577617e-01f,-5.519524589e-02f,-9.984755516e-01f,-7.450577617e-01f,-6.669999361e-01f,
9.939069748e-01f,-1.102222055e-01f,-1.102222055e-01f,-9.939069748e-01f,-9.939069748e-01f,1.102222055e-01f,1.102222055e-01f,9.939069748e-01f,
9.863080978e-01f,-1.649131179e-01f,-8.140363097e-01f,-5.808139443e-01f,1.649131179e-01f,9.863080978e-01f,5.808139443e-01f,-8.140363097e-01f,
5.000000000e-01f,5.000000000e-01f,5.000000000e-01f,-5.000000000e-01f,5.000000000e-01f,5.000000000e-01f,9.729399681e-01f,-2.310581058e-01f,
9.983015656e-01f,-5.825826526e-02f,6.647109985e-01f,-7.471005917e-01f,-5.825826526e-02f,-9.983015656e-01f,-7.471005917e-01f,-6.647109985e-01f,
9.932119250e-01f,-1.163186282e-01f,-1.163186282e-01f,-9.932119250e-01f,-9.932119250e-01f,1.163186282e-01f,1.163186282e-01f,9.932119250e-01f,
9.847484827e-01f,-1.739838719e-01f,-8.193475008e-01f,-5.732971430e-01f,1.739838719e-01f,9.847484827e-01f,5.732971430e-01f,-8.193475008e-01f,
5.000000000e-01f,5.000000000e-01f,5.000000000e-01f,-5.000000000e-01f,5.000000000e-01f,5.000000000e-01f,9.700312614e-01f,-2.429801822e-01f,
9.981181026e-01f,-6.132073700e-02f,6.624158025e-01f,-7.491363883e-01f,-6.132073700e-02f,-9.981181026e-01f,-7.491363883e-01f,-6.624158025e-01f,
9.924795628e-01f,-1.224106774e-01f,-1.224106774e-01f,-9.924795628e-01f,-9.924795628e-01f,1.224106774e-01f,1.224106774e-01f,9.924795628e-01f,
9.831054807e-01f,-1.830398887e-01f,-8.245893121e-01f,-5.657318234e-01f,1.830398887e-01f,9.831054807e-01f,5.657318234e-01f,-8.245893121e-01f,
5.000000000e-01f,5.000000000e-01f,5.000000000e-01f,-5.000000000e-01f,5.000000000e-01f,5.000000000e-01f,9.669764638e-01f,-2.548656464e-01f,
9.979252815e-01f,-6.438262761e-02f,6.601143479e-01f,-7.511651516e-01f,-6.438262761e-02f,-9.979252815e-01f,-7.511651516e-01f,-6.601143479e-01f,
9.917097688e-01f,-1.284981072e-01f,-1.284981072e-01f,-9.917097688e-01f,-9.917097688e-01f,1.284981072e-01f,1.284981072e-01f,9.917097688e-01f,
9.813792109e-01f,-1.920803934e-01f,-8.297612071e-01f,-5.581185222e-01f,1.920803934e-01f,9.813792109e-01f,5.581185222e-01f,-8.297612071e-01f,
5.000000000e-01f,5.000000000e-01f,5.000000000e-01f,-5.000000000e-01f,5.000000000e-01f,5.000000000e-01f,9.637760520e-01f,-2.667127550e-01f,
9.977230430e-01f,-6.744392216e-02f,6.578066945e-01f,-7.531868219e-01f,-6.744392216e-02f,-9.977230430e-01f,-7.531868219e-01f,-6.578066945e-01f,
9.909026623e-01f,-1.345807016e-01f,-1.345807016e-01f,-9.909026623e-01f,-9.909026623e-01f,1.345807016e-01f,1.345807016e-01f,9.909026623e-01f,
9.795697927e-01f,-2.011046410e-01f,-8.348628879e-01f,-5.504579544e-01f,2.011046410e-01f,9.795697927e-01f,5.504579544e-01f,-8.348628879e-01f,
5.000000000e-01f,5.000000000e-01f,5.000000000e-01f,-5.000000000e-01f,5.000000000e-01f,5.000000000e-01f,9.604305029e-01f,-2.785196900e-01f,
9.975114465e-01f,-7.050457597e-02f,6.554928422e-01f,-7.552013993e-01f,-7.050457597e-02f,-9.975114465e-01f,-7.552013993e-01f,-6.554928422e-01f,
9.900581837e-01f,-1.406582445e-01f,-1.406582445e-01f,-9.900581837e-01f,-9.900581837e-01f,1.406582445e-01f,1.406582445e-01f,9.900581837e-01f,
9.776773453e-01f,-2.101118416e-01f,-8.398938179e-01f,-5.427507758e-01f,2.101118416e-01f,9.776773453e-01f,5.427507758e-01f,-8.398938179e-01f,
5.000000000e-01f,5.000000000e-01f,5.000000000e-01f,-5.000000000e-01f,5.000000000e-01f,5.000000000e-01f,9.569403529e-01f,-2.902846634e-01f,
9.972904325e-01f,-7.356456667e-02f,6.531728506e-01f,-7.572088242e-01f,-7.356456667e-02f,-9.972904325e-01f,-7.572088242e-01f,-6.531728506e-01f,
9.891765118e-01f,-1.467304677e-01f,-1.467304677e-01f,-9.891765118e-01f,-9.891765118e-01f,1.467304677e-01f,1.467304677e-01f,9.891765118e-01f,
9.757021070e-01f,-2.191012353e-01f,-8.448535800e-01f,-5.349976420e-01f,2.191012353e-01f,9.757021070e-01f,5.349976420e-01f,-8.448535800e-01f,
5.000000000e-01f,5.000000000e-01f,5.000000000e-01f,-5.000000000e-01f,5.000000000e-01f,5.000000000e-01f,9.533060193e-01f,-3.020059466e-01f,
9.970600605e-01f,-7.662386447e-02f,6.508466601e-01f,-7.592092156e-01f,-7.662386447e-02f,-9.970600605e-01f,-7.592092156e-01f,-6.508466601e-01f,
9.882575870e-01f,-1.527971923e-01f,-1.527971923e-01f,-9.882575870e-01f,-9.882575870e-01f,1.527971923e-01f,1.527971923e-01f,9.882575870e-01f,
9.736442566e-01f,-2.280720770e-01f,-8.497417569e-01f,-5.271991491e-01f,2.280720770e-01f,9.736442566e-01f,5.271991491e-01f,-8.497417569e-01f,
5.000000000e-01f,5.000000000e-01f,5.000000000e-01f,-5.000000000e-01f,5.000000000e-01f,5.000000000e-01f,9.495281577e-01f,-3.136817515e-01f,
9.968202710e-01f,-7.968243957e-02f,6.485143900e-01f,-7.612023950e-01f,-7.968243957e-02f,-9.968202710e-01f,-7.612023950e-01f,-6.485143900e-01f,
9.873014092e-01f,-1.588581502e-01f,-1.588581502e-01f,-9.873014092e-01f,-9.873014092e-01f,1.588581502e-01f,1.588581502e-01f,9.873014092e-01f,
9.715039134e-01f,-2.370236069e-01f,-8.545579910e-01f,-5.193560123e-01f,2.370236069e-01f,9.715039134e-01f,5.193560123e-01f,-8.545579910e-01f,
5.000000000e-01f,5.000000000e-01f,5.000000000e-01f,-5.000000000e-01f,5.000000000e-01f,5.000000000e-01f,9.456073046e-01f,-3.253102899e-01f,
9.965711236e-01f,-8.274026215e-02f,6.461760402e-01f,-7.631884217e-01f,-8.274026215e-02f,-9.965711236e-01f,-7.631884217e-01f,-6.461760402e-01f,
9.863080978e-01f,-1.649131179e-01f,-1.649131179e-01f,-9.863080978e-01f,-9.863080978e-01f,1.649131179e-01f,1.649131179e-01f,9.863080978e-01f,
9.692812562e-01f,-2.459550500e-01f,-8.593018055e-01f,-5.114688277e-01f,2.459550500e-01f,9.692812562e-01f,5.114688277e-01f,-8.593018055e-01f,
5.000000000e-01f,5.000000000e-01f,5.000000000e-01f,-5.000000000e-01f,5.000000000e-01f,5.000000000e-01f,9.415440559e-01f,-3.368898630e-01f,
9.963126183e-01f,-8.579730988e-02f,6.438315511e-01f,-7.651672363e-01f,-8.579730988e-02f,-9.963126183e-01f,-7.651672363e-01f,-6.438315511e-01f,
9.852776527e-01f,-1.709618866e-01f,-1.709618866e-01f,-9.852776527e-01f,-9.852776527e-01f,1.709618866e-01f,1.709618866e-01f,9.852776527e-01f,
9.669764638e-01f,-2.548656464e-01f,-8.639728427e-01f,-5.035383701e-01f,2.548656464e-01f,9.669764638e-01f,5.035383701e-01f,-8.639728427e-01f,
5.000000000e-01f,5.000000000e-01f,5.000000000e-01f,-5.000000000e-01f,5.000000000e-01f,5.000000000e-01f,9.373390079e-01f,-3.484186828e-01f,
9.960446954e-01f,-8.885355294e-02f,6.414810419e-01f,-7.671388984e-01f,-8.885355294e-02f,-9.960446954e-01f,-7.671388984e-01f,-6.414810419e-01f,
9.842100739e-01f,-1.770042181e-01f,-1.770042181e-01f,-9.842100739e-01f,-9.842100739e-01f,1.770042181e-01f,1.770042181e-01f,9.842100739e-01f,
9.645897746e-01f,-2.637546659e-01f,-8.685706854e-01f,-4.955652654e-01f,2.637546659e-01f,9.645897746e-01f,4.955652654e-01f,-8.685706854e-01f,
5.000000000e-01f,5.000000000e-01f,5.000000000e-01f,-5.000000000e-01f,5.000000000e-01f,5.000000000e-01f,9.329928160e-01f,-3.598950505e-01f,
9.957674146e-01f,-9.190895408e-02f,6.391244531e-01f,-7.691033483e-01f,-9.190895408e-02f,-9.957674146e-01f,-7.691033483e-01f,-6.391244531e-01f,
9.831054807e-01f,-1.830398887e-01f,-1.830398887e-01f,-9.831054807e-01f,-9.831054807e-01f,1.830398887e-01f,1.830398887e-01f,9.831054807e-01f,
9.621214271e-01f,-2.726213634e-01f,-8.730949759e-01f,-4.875501692e-01f,2.726213634e-01f,9.621214271e-01f,4.875501692e-01f,-8.730949759e-01f,
5.000000000e-01f,5.000000000e-01f,5.000000000e-01f,-5.000000000e-01f,5.000000000e-01f,5.000000000e-01f,9.285060763e-01f,-3.713172078e-01f,
9.954807758e-01f,-9.496349841e-02f,6.367618442e-01f,-7.710605264e-01f,-9.496349841e-02f,-9.954807758e-01f,-7.710605264e-01f,-6.367618442e-01f,
9.819638729e-01f,-1.890686601e-01f,-1.890686601e-01f,-9.819638729e-01f,-9.819638729e-01f,1.890686601e-01f,1.890686601e-01f,9.819638729e-01f,
9.595715404e-01f,-2.814649343e-01f,-8.775452971e-01f,-4.794937670e-01f,2.814649343e-01f,9.595715404e-01f,4.794937670e-01f,-8.775452971e-01f,
5.000000000e-01f,5.000000000e-01f,5.000000000e-01f,-5.000000000e-01f,5.000000000e-01f,5.000000000e-01f,9.238795042e-01f,-3.826834261e-01f,
9.951847196e-01f,-9.801714122e-02f,6.343932748e-01f,-7.730104327e-01f,-9.801714122e-02f,-9.951847196e-01f,-7.730104327e-01f,-6.343932748e-01f,
9.807852507e-01f,-1.950903237e-01f,-1.950903237e-01f,-9.807852507e-01f,-9.807852507e-01f,1.950903237e-01f,1.950903237e-01f,9.807852507e-01f,
9.569403529e-01f,-2.902846634e-01f,-8.819212914e-01f,-4.713967443e-01f,2.902846634e-01f,9.569403529e-01f,4.713967443e-01f,-8.819212914e-01f,
5.000000000e-01f,5.000000000e-01f,5.000000000e-01f,-5.000000000e-01f,5.000000000e-01f,5.000000000e-01f,9.191138744e-01f,-3.939920366e-01f,
9.948793054e-01f,-1.010698602e-01f,6.320187449e-01f,-7.749531269e-01f,-1.010698602e-01f,-9.948793054e-01f,-7.749531269e-01f,-6.320187449e-01f,
9.795697927e-01f,-2.011046410e-01f,-2.011046410e-01f,-9.795697927e-01f,-9.795697927e-01f,2.011046410e-01f,2.011046410e-01f,9.795697927e-01f,
9.542281032e-01f,-2.990798354e-01f,-8.862225413e-01f,-4.632597864e-01f,2.990798354e-01f,9.542281032e-01f,4.632597864e-01f,-8.862225413e-01f,
5.000000000e-01f,5.000000000e-01f,5.000000000e-01f,-5.000000000e-01f,5.000000000e-01f,5.000000000e-01f,9.142097831e-01f,-4.052413106e-01f,
9.945645928e-01f,-1.041216329e-01f,6.296382546e-01f,-7.768884897e-01f,-1.041216329e-01f,-9.945645928e-01f,-7.768884897e-01f,-6.296382546e-01f,
9.783173800e-01f,-2.071113735e-01f,-2.071113735e-01f,-9.783173800e-01f,-9.783173800e-01f,2.071113735e-01f,2.071113735e-01f,9.783173800e-01f,
9.514350295e-01f,-3.078496456e-01f,-8.904487491e-01f,-4.550835788e-01f,3.078496456e-01f,9.514350295e-01f,4.550835788e-01f,-8.904487491e-01f,
5.000000000e-01f,5.000000000e-01f,5.000000000e-01f,-5.000000000e-01f,5.000000000e-01f,5.000000000e-01f,9.091680050e-01f,-4.164295495e-01f,
9.942404628e-01f,-1.071724221e-01f,6.272518039e-01f,-7.788165212e-01f,-1.071724221e-01f,-9.942404628e-01f,-7.788165212e-01f,-6.272518039e-01f,
9.770281315e-01f,-2.131103128e-01f,-2.131103128e-01f,-9.770281315e-01f,-9.770281315e-01f,2.131103128e-01f,2.131103128e-01f,9.770281315e-01f,
9.485613704e-01f,-3.165933788e-01f,-8.945994973e-01f,-4.468688369e-01f,3.165933788e-01f,9.485613704e-01f,4.468688369e-01f,-8.945994973e-01f,
5.000000000e-01f,5.000000000e-01f,5.000000000e-01f,-5.000000000e-01f,5.000000000e-01f,5.000000000e-01f,9.039893150e-01f,-4.275550842e-01f,
9.939069748e-01f,-1.102222055e-01f,6.248595119e-01f,-7.807372212e-01f,-1.102222055e-01f,-9.939069748e-01f,-7.807372212e-01f,-6.248595119e-01f,
9.757021070e-01f,-2.191012353e-01f,-2.191012353e-01f,-9.757021070e-01f,-9.757021070e-01f,2.191012353e-01f,2.191012353e-01f,9.757021070e-01f,
9.456073046e-01f,-3.253102899e-01f,-8.986744881e-01f,-4.386162460e-01f,3.253102899e-01f,9.456073046e-01f,4.386162460e-01f,-8.986744881e-01f,
5.000000000e-01f,5.000000000e-01f,5.000000000e-01f,-5.000000000e-01f,5.000000000e-01f,5.000000000e-01f,8.986744881e-01f,-4.386162460e-01f,
9.935641289e-01f,-1.132709533e-01f,6.224612594e-01f,-7.826505899e-01f,-1.132709533e-01f,-9.935641289e-01f,-7.826505899e-01f,-6.224612594e-01f,
9.743393660e-01f,-2.250839174e-01f,-2.250839174e-01f,-9.743393660e-01f,-9.743393660e-01f,2.250839174e-01f,2.250839174e-01f,9.743393660e-01f,
9.425731897e-01f,-3.339996636e-01f,-9.026733041e-01f,-4.303264916e-01f,3.339996636e-01f,9.425731897e-01f,4.303264916e-01f,-9.026733041e-01f,
5.000000000e-01f,5.000000000e-01f,5.000000000e-01f,-5.000000000e-01f,5.000000000e-01f,5.000000000e-01f,8.932242990e-01f,-4.496113360e-01f,
9.932119250e-01f,-1.163186282e-01f,6.200572252e-01f,-7.845565677e-01f,-1.163186282e-01f,-9.932119250e-01f,-7.845565677e-01f,-6.200572252e-01f,
9.729399681e-01f,-2.310581058e-01f,-2.310581058e-01f,-9.729399681e-01f,-9.729399681e-01f,2.310581058e-01f,2.310581058e-01f,9.729399681e-01f,
9.394592047e-01f,-3.426607251e-01f,-9.065957069e-01f,-4.220002592e-01f,3.426607251e-01f,9.394592047e-01f,4.220002592e-01f,-9.065957069e-01f,
5.000000000e-01f,5.000000000e-01f,5.000000000e-01f,-5.000000000e-01f,5.000000000e-01f,5.000000000e-01f,8.876396418e-01f,-4.605387151e-01f,
9.928504229e-01f,-1.193652153e-01f,6.176472902e-01f,-7.864552140e-01f,-1.193652153e-01f,-9.928504229e-01f,-7.864552140e-01f,-6.176472902e-01f,
9.715039134e-01f,-2.370236069e-01f,-2.370236069e-01f,-9.715039134e-01f,-9.715039134e-01f,2.370236069e-01f,2.370236069e-01f,9.715039134e-01f,
9.362656474e-01f,-3.512927592e-01f,-9.104412794e-01f,-4.136383235e-01f,3.512927592e-01f,9.362656474e-01f,4.136383235e-01f,-9.104412794e-01f,
5.000000000e-01f,5.000000000e-01f,5.000000000e-01f,-5.000000000e-01f,5.000000000e-01f,5.000000000e-01f,8.819212914e-01f,-4.713967443e-01f,
9.924795628e-01f,-1.224106774e-01f,6.152315736e-01f,-7.883464098e-01f,-1.224106774e-01f,-9.924795628e-01f,-7.883464098e-01f,-6.152315736e-01f,
9.700312614e-01f,-2.429801822e-01f,-2.429801822e-01f,-9.700312614e-01f,-9.700312614e-01f,2.429801822e-01f,2.429801822e-01f,9.700312614e-01f,
9.329928160e-01f,-3.598950505e-01f,-9.142097831e-01f,-4.052413106e-01f,3.598950505e-01f,9.329928160e-01f,4.052413106e-01f,-9.142097831e-01f,
5.000000000e-01f,5.000000000e-01f,5.000000000e-01f,-5.000000000e-01f,5.000000000e-01f,5.000000000e-01f,8.760700822e-01f,-4.821837842e-01f,
9.920992851e-01f,-1.254549772e-01f,6.128100753e-01f,-7.902302146e-01f,-1.254549772e-01f,-9.920992851e-01f,-7.902302146e-01f,-6.128100753e-01f,
9.685220718e-01f,-2.489276081e-01f,-2.489276081e-01f,-9.685220718e-01f,-9.685220718e-01f,2.489276081e-01f,2.489276081e-01f,9.685220718e-01f,
9.296408892e-01f,-3.684668243e-01f,-9.179008007e-01f,-3.968099952e-01f,3.684668243e-01f,9.296408892e-01f,3.968099952e-01f,-9.179008007e-01f,
5.000000000e-01f,5.000000000e-01f,5.000000000e-01f,-5.000000000e-01f,5.000000000e-01f,5.000000000e-01f,8.700869679e-01f,-4.928981960e-01f,
9.917097688e-01f,-1.284981072e-01f,6.103827953e-01f,-7.921065688e-01f,-1.284981072e-01f,-9.917097688e-01f,-7.921065688e-01f,-6.103827953e-01f,
9.669764638e-01f,-2.548656464e-01f,-2.548656464e-01f,-9.669764638e-01f,-9.669764638e-01f,2.548656464e-01f,2.548656464e-01f,9.669764638e-01f,
9.262102246e-01f,-3.770074248e-01f,-9.215140343e-01f,-3.883450329e-01f,3.770074248e-01f,9.262102246e-01f,3.883450329e-01f,-9.215140343e-01f,
5.000000000e-01f,5.000000000e-01f,5.000000000e-01f,-5.000000000e-01f,5.000000000e-01f,5.000000000e-01f,8.639728427e-01f,-5.035383701e-01f,
9.913108349e-01f,-1.315400302e-01f,6.079497933e-01f,-7.939754725e-01f,-1.315400302e-01f,-9.913108349e-01f,-7.939754725e-01f,-6.079497933e-01f,
9.653944373e-01f,-2.607941031e-01f,-2.607941031e-01f,-9.653944373e-01f,-9.653944373e-01f,2.607941031e-01f,2.607941031e-01f,9.653944373e-01f,
9.227011204e-01f,-3.855160475e-01f,-9.250492454e-01f,-3.798471987e-01f,3.855160475e-01f,9.227011204e-01f,3.798471987e-01f,-9.250492454e-01f,
5.000000000e-01f,5.000000000e-01f,5.000000000e-01f,-5.000000000e-01f,5.000000000e-01f,5.000000000e-01f,8.577286005e-01f,-5.141027570e-01f,
9.909026623e-01f,-1.345807016e-01f,6.055110693e-01f,-7.958369255e-01f,-1.345807016e-01f,-9.909026623e-01f,-7.958369255e-01f,-6.055110693e-01f,
9.637760520e-01f,-2.667127550e-01f,-2.667127550e-01f,-9.637760520e-01f,-9.637760520e-01f,2.667127550e-01f,2.667127550e-01f,9.637760520e-01f,
9.191138744e-01f,-3.939920366e-01f,-9.285060763e-01f,-3.713172078e-01f,3.939920366e-01f,9.191138744e-01f,3.713172078e-01f,-9.285060763e-01f,
5.000000000e-01f,5.000000000e-01f,5.000000000e-01f,-5.000000000e-01f,5.000000000e-01f,5.000000000e-01f,8.513551950e-01f,-5.245896578e-01f,
9.904850721e-01f,-1.376201212e-01f,6.030666232e-01f,-7.976908684e-01f,-1.376201212e-01f,-9.904850721e-01f,-7.976908684e-01f,-6.030666232e-01f,
9.621214271e-01f,-2.726213634e-01f,-2.726213634e-01f,-9.621214271e-01f,-9.621214271e-01f,2.726213634e-01f,2.726213634e-01f,9.621214271e-01f,
9.154487252e-01f,-4.024346471e-01f,-9.318842888e-01f,-3.627557158e-01f,4.024346471e-01f,9.154487252e-01f,3.627557158e-01f,-9.318842888e-01f,
5.000000000e-01f,5.000000000e-01f,5.000000000e-01f,-5.000000000e-01f,5.000000000e-01f,5.000000000e-01f,8.448535800e-01f,-5.349976420e-01f,
9.900581837e-01f,-1.406582445e-01f,6.006164551e-01f,-7.995372415e-01f,-1.406582445e-01f,-9.900581837e-01f,-7.995372415e-01f,-6.006164551e-01f,
9.604305029e-01f,-2.785196900e-01f,-2.785196900e-01f,-9.604305029e-01f,-9.604305029e-01f,2.785196900e-01f,2.785196900e-01f,9.604305029e-01f,
9.117060304e-01f,-4.108431637e-01f,-9.351835251e-01f,-3.541635275e-01f,4.108431637e-01f,9.117060304e-01f,3.541635275e-01f,-9.351835251e-01f,
5.000000000e-01f,5.000000000e-01f,5.000000000e-01f,-5.000000000e-01f,5.000000000e-01f,5.000000000e-01f,8.382247090e-01f,-5.453249812e-01f,
9.896219969e-01f,-1.436950266e-01f,5.981606841e-01f,-8.013761640e-01f,-1.436950266e-01f,-9.896219969e-01f,-8.013761640e-01f,-5.981606841e-01f,
9.587034583e-01f,-2.844075263e-01f,-2.844075263e-01f,-9.587034583e-01f,-9.587034583e-01f,2.844075263e-01f,2.844075263e-01f,9.587034583e-01f,
9.078860879e-01f,-4.192169011e-01f,-9.384035468e-01f,-3.455413282e-01f,4.192169011e-01f,9.078860879e-01f,3.455413282e-01f,-9.384035468e-01f,
5.000000000e-01f,5.000000000e-01f,5.000000000e-01f,-5.000000000e-01f,5.000000000e-01f,5.000000000e-01f,8.314695954e-01f,-5.555702448e-01f,
9.891765118e-01f,-1.467304677e-01f,5.956993103e-01f,-8.032075167e-01f,-1.467304677e-01f,-9.891765118e-01f,-8.032075167e-01f,-5.956993103e-01f,
9.569403529e-01f,-2.902846634e-01f,-2.902846634e-01f,-9.569403529e-01f,-9.569403529e-01f,2.902846634e-01f,2.902846634e-01f,9.569403529e-01f,
9.039893150e-01f,-4.275550842e-01f,-9.415440559e-01f,-3.368898630e-01f,4.275550842e-01f,9.039893150e-01f,3.368898630e-01f,-9.415440559e-01f,
5.000000000e-01f,5.000000000e-01f,5.000000000e-01f,-5.000000000e-01f,5.000000000e-01f,5.000000000e-01f,8.245893121e-01f,-5.657318234e-01f,
9.887216687e-01f,-1.497645378e-01f,5.932322741e-01f,-8.050313592e-01f,-1.497645378e-01f,-9.887216687e-01f,-8.050313592e-01f,-5.932322741e-01f,
9.551411867e-01f,-2.961508930e-01f,-2.961508930e-01f,-9.551411867e-01f,-9.551411867e-01f,2.961508930e-01f,2.961508930e-01f,9.551411867e-01f,
9.000158906e-01f,-4.358570874e-01f,-9.446048141e-01f,-3.282098472e-01f,4.358570874e-01f,9.000158906e-01f,3.282098472e-01f,-9.446048141e-01f,
5.000000000e-01f,5.000000000e-01f,5.000000000e-01f,-5.000000000e-01f,5.000000000e-01f,5.000000000e-01f,8.175848126e-01f,-5.758081675e-01f,
9.882575870e-01f,-1.527971923e-01f,5.907596946e-01f,-8.068475723e-01f,-1.527971923e-01f,-9.882575870e-01f,-8.068475723e-01f,-5.907596946e-01f,
9.533060193e-01f,-3.020059466e-01f,-3.020059466e-01f,-9.533060193e-01f,-9.533060193e-01f,3.020059466e-01f,3.020059466e-01f,9.533060193e-01f,
8.959662318e-01f,-4.441221356e-01f,-9.475855827e-01f,-3.195020258e-01f,4.441221356e-01f,8.959662318e-01f,3.195020258e-01f,-9.475855827e-01f,
5.000000000e-01f,5.000000000e-01f,5.000000000e-01f,-5.000000000e-01f,5.000000000e-01f,5.000000000e-01f,8.104571700e-01f,-5.857978463e-01f,
9.877841473e-01f,-1.558284014e-01f,5.882815719e-01f,-8.086561561e-01f,-1.558284014e-01f,-9.877841473e-01f,-8.086561561e-01f,-5.882815719e-01f,
9.514350295e-01f,-3.078496456e-01f,-3.078496456e-01f,-9.514350295e-01f,-9.514350295e-01f,3.078496456e-01f,3.078496456e-01f,9.514350295e-01f,
8.918406963e-01f,-4.523495734e-01f,-9.504860640e-01f,-3.107671440e-01f,4.523495734e-01f,8.918406963e-01f,3.107671440e-01f,-9.504860640e-01f,
5.000000000e-01f,5.000000000e-01f,5.000000000e-01f,-5.000000000e-01f,5.000000000e-01f,5.000000000e-01f,8.032075167e-01f,-5.956993103e-01f,
9.873014092e-01f,-1.588581502e-01f,5.857978463e-01f,-8.104571700e-01f,-1.588581502e-01f,-9.873014092e-01f,-8.104571700e-01f,-5.857978463e-01f,
9.495281577e-01f,-3.136817515e-01f,-3.136817515e-01f,-9.495281577e-01f,-9.495281577e-01f,3.136817515e-01f,3.136817515e-01f,9.495281577e-01f,
8.876396418e-01f,-4.605387151e-01f,-9.533060193e-01f,-3.020059466e-01f,4.605387151e-01f,8.876396418e-01f,3.020059466e-01f,-9.533060193e-01f,
5.000000000e-01f,5.000000000e-01f,5.000000000e-01f,-5.000000000e-01f,5.000000000e-01f,5.000000000e-01f,7.958369255e-01f,-6.055110693e-01f,
9.868093729e-01f,-1.618863940e-01f,5.833086371e-01f,-8.122506142e-01f,-1.618863940e-01f,-9.868093729e-01f,-8.122506142e-01f,-5.833086371e-01f,
9.475855827e-01f,-3.195020258e-01f,-3.195020258e-01f,-9.475855827e-01f,-9.475855827e-01f,3.195020258e-01f,3.195020258e-01f,9.475855827e-01f,
8.833633661e-01f,-4.686888158e-01f,-9.560452700e-01f,-2.932191491e-01f,4.686888158e-01f,8.833633661e-01f,2.932191491e-01f,-9.560452700e-01f,
5.000000000e-01f,5.000000000e-01f,5.000000000e-01f,-5.000000000e-01f,5.000000000e-01f,5.000000000e-01f,7.883464098e-01f,-6.152315736e-01f,
9.863080978e-01f,-1.649131179e-01f,5.808139443e-01f,-8.140363097e-01f,-1.649131179e-01f,-9.863080978e-01f,-8.140363097e-01f,-5.808139443e-01f,
9.456073046e-01f,-3.253102899e-01f,-3.253102899e-01f,-9.456073046e-01f,-9.456073046e-01f,3.253102899e-01f,3.253102899e-01f,9.456073046e-01f,
8.790122271e-01f,-4.767992198e-01f,-9.587034583e-01f,-2.844075263e-01f,4.767992198e-01f,8.790122271e-01f,2.844075263e-01f,-9.587034583e-01f,
5.000000000e-01f,5.000000000e-01f,5.000000000e-01f,-5.000000000e-01f,5.000000000e-01f,5.000000000e-01f,7.807372212e-01f,-6.248595119e-01f,
9.857975245e-01f,-1.679382920e-01f,5.783137679e-01f,-8.158144355e-01f,-1.679382920e-01f,-9.857975245e-01f,-8.158144355e-01f,-5.783137679e-01f,
9.435934424e-01f,-3.311063051e-01f,-3.311063051e-01f,-9.435934424e-01f,-9.435934424e-01f,3.311063051e-01f,3.311063051e-01f,9.435934424e-01f,
8.745866418e-01f,-4.848692417e-01f,-9.612804651e-01f,-2.755718231e-01f,4.848692417e-01f,8.745866418e-01f,2.755718231e-01f,-9.612804651e-01f,
5.000000000e-01f,5.000000000e-01f,5.000000000e-01f,-5.000000000e-01f,5.000000000e-01f,5.000000000e-01f,7.730104327e-01f,-6.343932748e-01f,
9.852776527e-01f,-1.709618866e-01f,5.758081675e-01f,-8.175848126e-01f,-1.709618866e-01f,-9.852776527e-01f,-8.175848126e-01f,-5.758081675e-01f,
9.415440559e-01f,-3.368898630e-01f,-3.368898630e-01f,-9.415440559e-01f,-9.415440559e-01f,3.368898630e-01f,3.368898630e-01f,9.415440559e-01f,
8.700869679e-01f,-4.928981960e-01f,-9.637760520e-01f,-2.667127550e-01f,4.928981960e-01f,8.700869679e-01f,2.667127550e-01f,-9.637760520e-01f,
5.000000000e-01f,5.000000000e-01f,5.000000000e-01f,-5.000000000e-01f,5.000000000e-01f,5.000000000e-01f,7.651672363e-01f,-6.438315511e-01f,
9.847484827e-01f,-1.739838719e-01f,5.732971430e-01f,-8.193475008e-01f,-1.739838719e-01f,-9.847484827e-01f,-8.193475008e-01f,-5.732971430e-01f,
9.394592047e-01f,-3.426607251e-01f,-3.426607251e-01f,-9.394592047e-01f,-9.394592047e-01f,3.426607251e-01f,3.426607251e-01f,9.394592047e-01f,
8.655136228e-01f,-5.008853674e-01f,-9.661899805e-01f,-2.578310966e-01f,5.008853674e-01f,8.655136228e-01f,2.578310966e-01f,-9.661899805e-01f,
5.000000000e-01f,5.000000000e-01f,5.000000000e-01f,-5.000000000e-01f,5.000000000e-01f,5.000000000e-01f,7.572088242e-01f,-6.531728506e-01f,
9.842100739e-01f,-1.770042181e-01f,5.707807541e-01f,-8.211025000e-01f,-1.770042181e-01f,-9.842100739e-01f,-8.211025000e-01f,-5.707807541e-01f,
9.373390079e-01f,-3.484186828e-01f,-3.484186828e-01f,-9.373390079e-01f,-9.373390079e-01f,3.484186828e-01f,3.484186828e-01f,9.373390079e-01f,
8.608669639e-01f,-5.088301301e-01f,-9.685220718e-01f,-2.489276081e-01f,5.088301301e-01f,8.608669639e-01f,2.489276081e-01f,-9.685220718e-01f,
5.000000000e-01f,5.000000000e-01f,5.000000000e-01f,-5.000000000e-01f,5.000000000e-01f,5.000000000e-01f,7.491363883e-01f,-6.624158025e-01f,
9.836624265e-01f,-1.800228953e-01f,5.682589412e-01f,-8.228498101e-01f,-1.800228953e-01f,-9.836624265e-01f,-8.228498101e-01f,-5.682589412e-01f,
9.351835251e-01f,-3.541635275e-01f,-3.541635275e-01f,-9.351835251e-01f,-9.351835251e-01f,3.541635275e-01f,3.541635275e-01f,9.351835251e-01f,
8.561473489e-01f,-5.167317986e-01f,-9.707721472e-01f,-2.400030196e-01f,5.167317986e-01f,8.561473489e-01f,2.400030196e-01f,-9.707721472e-01f,
5.000000000e-01f,5.000000000e-01f,5.000000000e-01f,-5.000000000e-01f,5.000000000e-01f,5.000000000e-01f,7.409511209e-01f,-6.715589762e-01f,
9.831054807e-01f,-1.830398887e-01f,5.657318234e-01f,-8.245893121e-01f,-1.830398887e-01f,-9.831054807e-01f,-8.245893121e-01f,-5.657318234e-01f,
9.329928160e-01f,-3.598950505e-01f,-3.598950505e-01f,-9.329928160e-01f,-9.329928160e-01f,3.598950505e-01f,3.598950505e-01f,9.329928160e-01f,
8.513551950e-01f,-5.245896578e-01f,-9.729399681e-01f,-2.310581058e-01f,5.245896578e-01f,8.513551950e-01f,2.310581058e-01f,-9.729399681e-01f,
5.000000000e-01f,5.000000000e-01f,5.000000000e-01f,-5.000000000e-01f,5.000000000e-01f,5.000000000e-01f,7.326542735e-01f,-6.806010008e-01f,
9.825392962e-01f,-1.860551536e-01f,5.631993413e-01f,-8.263210654e-01f,-1.860551536e-01f,-9.825392962e-01f,-8.263210654e-01f,-5.631993413e-01f,
9.307669401e-01f,-3.656129837e-01f,-3.656129837e-01f,-9.307669401e-01f,-9.307669401e-01f,3.656129837e-01f,3.656129837e-01f,9.307669401e-01f,
8.464909196e-01f,-5.324031115e-01f,-9.750253558e-01f,-2.220936269e-01f,5.324031115e-01f,8.464909196e-01f,2.220936269e-01f,-9.750253558e-01f,
5.000000000e-01f,5.000000000e-01f,5.000000000e-01f,-5.000000000e-01f,5.000000000e-01f,5.000000000e-01f,7.242470980e-01f,-6.895405650e-01f,
9.819638729e-01f,-1.890686601e-01f,5.606615543e-01f,-8.280450702e-01f,-1.890686601e-01f,-9.819638729e-01f,-8.280450702e-01f,-5.606615543e-01f,
9.285060763e-01f,-3.713172078e-01f,-3.713172078e-01f,-9.285060763e-01f,-9.285060763e-01f,3.713172078e-01f,3.713172078e-01f,9.285060763e-01f,
8.415549994e-01f,-5.401714444e-01f,-9.770281315e-01f,-2.131103128e-01f,5.401714444e-01f,8.415549994e-01f,2.131103128e-01f,-9.770281315e-01f,
5.000000000e-01f,5.000000000e-01f,5.000000000e-01f,-5.000000000e-01f,5.000000000e-01f,5.000000000e-01f,7.157308459e-01f,-6.983762383e-01f,
9.813792109e-01f,-1.920803934e-01f,5.581185222e-01f,-8.297612071e-01f,-1.920803934e-01f,-9.813792109e-01f,-8.297612071e-01f,-5.581185222e-01f,
9.262102246e-01f,-3.770074248e-01f,-3.770074248e-01f,-9.262102246e-01f,-9.262102246e-01f,3.770074248e-01f,3.770074248e-01f,9.262102246e-01f,
8.365477324e-01f,-5.478940606e-01f,-9.789481759e-01f,-2.041089684e-01f,5.478940606e-01f,8.365477324e-01f,2.041089684e-01f,-9.789481759e-01f,
5.000000000e-01f,5.000000000e-01f,5.000000000e-01f,-5.000000000e-01f,5.000000000e-01f,5.000000000e-01f,7.071067691e-01f,-7.071067691e-01f,
9.807852507e-01f,-1.950903237e-01f,5.555702448e-01f,-8.314695954e-01f,-1.950903237e-01f,-9.807852507e-01f,-8.314695954e-01f,-5.555702448e-01f,
9.238795042e-01f,-3.826834261e-01f,-3.826834261e-01f,-9.238795042e-01f,-9.238795042e-01f,3.826834261e-01f,3.826834261e-01f,9.238795042e-01f,
8.314695954e-01f,-5.555702448e-01f,-9.807852507e-01f,-1.950903237e-01f,5.555702448e-01f,8.314695954e-01f,1.950903237e-01f,-9.807852507e-01f,
5.000000000e-01f,5.000000000e-01f,5.000000000e-01f,-5.000000000e-01f,5.000000000e-01f,5.000000000e-01f,6.983762383e-01f,-7.157308459e-01f,
9.801821113e-01f,-1.980984062e-01f,5.530167222e-01f,-8.331701756e-01f,-1.980984062e-01f,-9.801821113e-01f,-8.331701756e-01f,-5.530167222e-01f,
9.215140343e-01f,-3.883450329e-01f,-3.883450329e-01f,-9.215140343e-01f,-9.215140343e-01f,3.883450329e-01f,3.883450329e-01f,9.215140343e-01f,
8.263210654e-01f,-5.631993413e-01f,-9.825392962e-01f,-1.860551536e-01f,5.631993413e-01f,8.263210654e-01f,1.860551536e-01f,-9.825392962e-01f,
5.000000000e-01f,5.000000000e-01f,5.000000000e-01f,-5.000000000e-01f,5.000000000e-01f,5.000000000e-01f,6.895405650e-01f,-7.242470980e-01f,
9.795697927e-01f,-2.011046410e-01f,5.504579544e-01f,-8.348628879e-01f,-2.011046410e-01f,-9.795697927e-01f,-8.348628879e-01f,-5.504579544e-01f,
9.191138744e-01f,-3.939920366e-01f,-3.939920366e-01f,-9.191138744e-01f,-9.191138744e-01f,3.939920366e-01f,3.939920366e-01f,9.191138744e-01f,
8.211025000e-01f,-5.707807541e-01f,-9.842100739e-01f,-1.770042181e-01f,5.707807541e-01f,8.211025000e-01f,1.770042181e-01f,-9.842100739e-01f,
5.000000000e-01f,5.000000000e-01f,5.000000000e-01f,-5.000000000e-01f,5.000000000e-01f,5.000000000e-01f,6.806010008e-01f,-7.326542735e-01f,
9.789481759e-01f,-2.041089684e-01f,5.478940606e-01f,-8.365477324e-01f,-2.041089684e-01f,-9.789481759e-01f,-8.365477324e-01f,-5.478940606e-01f,
9.166790843e-01f,-3.996241987e-01f,-3.996241987e-01f,-9.166790843e-01f,-9.166790843e-01f,3.996241987e-01f,3.996241987e-01f,9.166790843e-01f,
8.158144355e-01f,-5.783137679e-01f,-9.857975245e-01f,-1.679382920e-01f,5.783137679e-01f,8.158144355e-01f,1.679382920e-01f,-9.857975245e-01f,
5.000000000e-01f,5.000000000e-01f,5.000000000e-01f,-5.000000000e-01f,5.000000000e-01f,5.000000000e-01f,6.715589762e-01f,-7.409511209e-01f,
9.783173800e-01f,-2.071113735e-01f,5.453249812e-01f,-8.382247090e-01f,-2.071113735e-01f,-9.783173800e-01f,-8.382247090e-01f,-5.453249812e-01f,
9.142097831e-01f,-4.052413106e-01f,-4.052413106e-01f,-9.142097831e-01f,-9.142097831e-01f,4.052413106e-01f,4.052413106e-01f,9.142097831e-01f,
8.104571700e-01f,-5.857978463e-01f,-9.873014092e-01f,-1.588581502e-01f,5.857978463e-01f,8.104571700e-01f,1.588581502e-01f,-9.873014092e-01f,
5.000000000e-01f,5.000000000e-01f,5.000000000e-01f,-5.000000000e-01f,5.000000000e-01f,5.000000000e-01f,6.624158025e-01f,-7.491363883e-01f,
9.776773453e-01f,-2.101118416e-01f,5.427507758e-01f,-8.398938179e-01f,-2.101118416e-01f,-9.776773453e-01f,-8.398938179e-01f,-5.427507758e-01f,
9.117060304e-01f,-4.108431637e-01f,-4.108431637e-01f,-9.117060304e-01f,-9.117060304e-01f,4.108431637e-01f,4.108431637e-01f,9.117060304e-01f,
8.050313592e-01f,-5.932322741e-01f,-9.887216687e-01f,-1.497645378e-01f,5.932322741e-01f,8.050313592e-01f,1.497645378e-01f,-9.887216687e-01f,
5.000000000e-01f,5.000000000e-01f,5.000000000e-01f,-5.000000000e-01f,5.000000000e-01f,5.000000000e-01f,6.531728506e-01f,-7.572088242e-01f,
9.770281315e-01f,-2.131103128e-01f,5.401714444e-01f,-8.415549994e-01f,-2.131103128e-01f,-9.770281315e-01f,-8.415549994e-01f,-5.401714444e-01f,
9.091680050e-01f,-4.164295495e-01f,-4.164295495e-01f,-9.091680050e-01f,-9.091680050e-01f,4.164295495e-01f,4.164295495e-01f,9.091680050e-01f,
7.995372415e-01f,-6.006164551e-01f,-9.900581837e-01f,-1.406582445e-01f,6.006164551e-01f,7.995372415e-01f,1.406582445e-01f,-9.900581837e-01f,
5.000000000e-01f,5.000000000e-01f,5.000000000e-01f,-5.000000000e-01f,5.000000000e-01f,5.000000000e-01f,6.438315511e-01f,-7.651672363e-01f,
9.763697386e-01f,-2.161068022e-01f,5.375870466e-01f,-8.432082534e-01f,-2.161068022e-01f,-9.763697386e-01f,-8.432082534e-01f,-5.375870466e-01f,
9.065957069e-01f,-4.220002592e-01f,-4.220002592e-01f,-9.065957069e-01f,-9.065957069e-01f,4.220002592e-01f,4.220002592e-01f,9.065957069e-01f,
7.939754725e-01f,-6.079497933e-01f,-9.913108349e-01f,-1.315400302e-01f,6.079497933e-01f,7.939754725e-01f,1.315400302e-01f,-9.913108349e-01f,
5.000000000e-01f,5.000000000e-01f,5.000000000e-01f,-5.000000000e-01f,5.000000000e-01f,5.000000000e-01f,6.343932748e-01f,-7.730104327e-01f,
9.757021070e-01f,-2.191012353e-01f,5.349976420e-01f,-8.448535800e-01f,-2.191012353e-01f,-9.757021070e-01f,-8.448535800e-01f,-5.349976420e-01f,
9.039893150e-01f,-4.275550842e-01f,-4.275550842e-01f,-9.039893150e-01f,-9.039893150e-01f,4.275550842e-01f,4.275550842e-01f,9.039893150e-01f,
7.883464098e-01f,-6.152315736e-01f,-9.924795628e-01f,-1.224106774e-01f,6.152315736e-01f,7.883464098e-01f,1.224106774e-01f,-9.924795628e-01f,
5.000000000e-01f,5.000000000e-01f,5.000000000e-01f,-5.000000000e-01f,5.000000000e-01f,5.000000000e-01f,6.248595119e-01f,-7.807372212e-01f,
9.750253558e-01f,-2.220936269e-01f,5.324031115e-01f,-8.464909196e-01f,-2.220936269e-01f,-9.750253558e-01f,-8.464909196e-01f,-5.324031115e-01f,
9.013488293e-01f,-4.330938160e-01f,-4.330938160e-01f,-9.013488293e-01f,-9.013488293e-01f,4.330938160e-01f,4.330938160e-01f,9.013488293e-01f,
7.826505899e-01f,-6.224612594e-01f,-9.935641289e-01f,-1.132709533e-01f,6.224612594e-01f,7.826505899e-01f,1.132709533e-01f,-9.935641289e-01f,
5.000000000e-01f,5.000000000e-01f,5.000000000e-01f,-5.000000000e-01f,5.000000000e-01f,5.000000000e-01f,6.152315736e-01f,-7.883464098e-01f,
9.743393660e-01f,-2.250839174e-01f,5.298036337e-01f,-8.481203318e-01f,-2.250839174e-01f,-9.743393660e-01f,-8.481203318e-01f,-5.298036337e-01f,
8.986744881e-01f,-4.386162460e-01f,-4.386162460e-01f,-8.986744881e-01f,-8.986744881e-01f,4.386162460e-01f,4.386162460e-01f,8.986744881e-01f,
7.768884897e-01f,-6.296382546e-01f,-9.945645928e-01f,-1.041216329e-01f,6.296382546e-01f,7.768884897e-01f,1.041216329e-01f,-9.945645928e-01f,
5.000000000e-01f,5.000000000e-01f,5.000000000e-01f,-5.000000000e-01f,5.000000000e-01f,5.000000000e-01f,6.055110693e-01f,-7.958369255e-01f,
9.736442566e-01f,-2.280720770e-01f,5.271991491e-01f,-8.497417569e-01f,-2.280720770e-01f,-9.736442566e-01f,-8.497417569e-01f,-5.271991491e-01f,
8.959662318e-01f,-4.441221356e-01f,-4.441221356e-01f,-8.959662318e-01f,-8.959662318e-01f,4.441221356e-01f,4.441221356e-01f,8.959662318e-01f,
7.710605264e-01f,-6.367618442e-01f,-9.954807758e-01f,-9.496349841e-02f,6.367618442e-01f,7.710605264e-01f,9.496349841e-02f,-9.954807758e-01f,
5.000000000e-01f,5.000000000e-01f,5.000000000e-01f,-5.000000000e-01f,5.000000000e-01f,5.000000000e-01f,5.956993103e-01f,-8.032075167e-01f,
9.729399681e-01f,-2.310581058e-01f,5.245896578e-01f,-8.513551950e-01f,-2.310581058e-01f,-9.729399681e-01f,-8.513551950e-01f,-5.245896578e-01f,
8.932242990e-01f,-4.496113360e-01f,-4.496113360e-01f,-8.932242990e-01f,-8.932242990e-01f,4.496113360e-01f,4.496113360e-01f,8.932242990e-01f,
7.651672363e-01f,-6.438315511e-01f,-9.963126183e-01f,-8.579730988e-02f,6.438315511e-01f,7.651672363e-01f,8.579730988e-02f,-9.963126183e-01f,
5.000000000e-01f,5.000000000e-01f,5.000000000e-01f,-5.000000000e-01f,5.000000000e-01f,5.000000000e-01f,5.857978463e-01f,-8.104571700e-01f,
9.722265005e-01f,-2.340419590e-01f,5.219752789e-01f,-8.529605865e-01f,-2.340419590e-01f,-9.722265005e-01f,-8.529605865e-01f,-5.219752789e-01f,
8.904487491e-01f,-4.550835788e-01f,-4.550835788e-01f,-8.904487491e-01f,-8.904487491e-01f,4.550835788e-01f,4.550835788e-01f,8.904487491e-01f,
7.592092156e-01f,-6.508466601e-01f,-9.970600605e-01f,-7.662386447e-02f,6.508466601e-01f,7.592092156e-01f,7.662386447e-02f,-9.970600605e-01f,
5.000000000e-01f,5.000000000e-01f,5.000000000e-01f,-5.000000000e-01f,5.000000000e-01f,5.000000000e-01f,5.758081675e-01f,-8.175848126e-01f,
9.715039134e-01f,-2.370236069e-01f,5.193560123e-01f,-8.545579910e-01f,-2.370236069e-01f,-9.715039134e-01f,-8.545579910e-01f,-5.193560123e-01f,
8.876396418e-01f,-4.605387151e-01f,-4.605387151e-01f,-8.876396418e-01f,-8.876396418e-01f,4.605387151e-01f,4.605387151e-01f,8.876396418e-01f,
7.531868219e-01f,-6.578066945e-01f,-9.977230430e-01f,-6.744392216e-02f,6.578066945e-01f,7.531868219e-01f,6.744392216e-02f,-9.977230430e-01f,
5.000000000e-01f,5.000000000e-01f,5.000000000e-01f,-5.000000000e-01f,5.000000000e-01f,5.000000000e-01f,5.657318234e-01f,-8.245893121e-01f,
9.707721472e-01f,-2.400030196e-01f,5.167317986e-01f,-8.561473489e-01f,-2.400030196e-01f,-9.707721472e-01f,-8.561473489e-01f,-5.167317986e-01f,
8.847970963e-01f,-4.659765065e-01f,-4.659765065e-01f,-8.847970963e-01f,-8.847970963e-01f,4.659765065e-01f,4.659765065e-01f,8.847970963e-01f,
7.471005917e-01f,-6.647109985e-01f,-9.983015656e-01f,-5.825826526e-02f,6.647109985e-01f,7.471005917e-01f,5.825826526e-02f,-9.983015656e-01f,
5.000000000e-01f,5.000000000e-01f,5.000000000e-01f,-5.000000000e-01f,5.000000000e-01f,5.000000000e-01f,5.555702448e-01f,-8.314695954e-01f,
9.700312614e-01f,-2.429801822e-01f,5.141027570e-01f,-8.577286005e-01f,-2.429801822e-01f,-9.700312614e-01f,-8.577286005e-01f,-5.141027570e-01f,
8.819212914e-01f,-4.713967443e-01f,-4.713967443e-01f,-8.819212914e-01f,-8.819212914e-01f,4.713967443e-01f,4.713967443e-01f,8.819212914e-01f,
7.409511209e-01f,-6.715589762e-01f,-9.987954497e-01f,-4.906767607e-02f,6.715589762e-01f,7.409511209e-01f,4.906767607e-02f,-9.987954497e-01f,
5.000000000e-01f,5.000000000e-01f,5.000000000e-01f,-5.000000000e-01f,5.000000000e-01f,5.000000000e-01f,5.453249812e-01f,-8.382247090e-01f,
9.692812562e-01f,-2.459550500e-01f,5.114688277e-01f,-8.593018055e-01f,-2.459550500e-01f,-9.692812562e-01f,-8.593018055e-01f,-5.114688277e-01f,
8.790122271e-01f,-4.767992198e-01f,-4.767992198e-01f,-8.790122271e-01f,-8.790122271e-01f,4.767992198e-01f,4.767992198e-01f,8.790122271e-01f,
7.347388864e-01f,-6.783500314e-01f,-9.992047548e-01f,-3.987292573e-02f,6.783500314e-01f,7.347388864e-01f,3.987292573e-02f,-9.992047548e-01f,
5.000000000e-01f,5.000000000e-01f,5.000000000e-01f,-5.000000000e-01f,5.000000000e-01f,5.000000000e-01f,5.349976420e-01f,-8.448535800e-01f,
9.685220718e-01f,-2.489276081e-01f,5.088301301e-01f,-8.608669639e-01f,-2.489276081e-01f,-9.685220718e-01f,-8.608669639e-01f,-5.088301301e-01f,
8.760700822e-01f,-4.821837842e-01f,-4.821837842e-01f,-8.760700822e-01f,-8.760700822e-01f,4.821837842e-01f,4.821837842e-01f,8.760700822e-01f,
7.284643650e-01f,-6.850836873e-01f,-9.995294213e-01f,-3.067480400e-02f,6.850836873e-01f,7.284643650e-01f,3.067480400e-02f,-9.995294213e-01f,
5.000000000e-01f,5.000000000e-01f,5.000000000e-01f,-5.000000000e-01f,5.000000000e-01f,5.000000000e-01f,5.245896578e-01f,-8.513551950e-01f,
9.677538276e-01f,-2.518978119e-01f,5.061866641e-01f,-8.624239564e-01f,-2.518978119e-01f,-9.677538276e-01f,-8.624239564e-01f,-5.061866641e-01f,
8.730949759e-01f,-4.875501692e-01f,-4.875501692e-01f,-8.730949759e-01f,-8.730949759e-01f,4.875501692e-01f,4.875501692e-01f,8.730949759e-01f,
7.221282125e-01f,-6.917592287e-01f,-9.997693896e-01f,-2.147408016e-02f,6.917592287e-01f,7.221282125e-01f,2.147408016e-02f,-9.997693896e-01f,
5.000000000e-01f,5.000000000e-01f,5.000000000e-01f,-5.000000000e-01f,5.000000000e-01f,5.000000000e-01f,5.141027570e-01f,-8.577286005e-01f,
9.669764638e-01f,-2.548656464e-01f,5.035383701e-01f,-8.639728427e-01f,-2.548656464e-01f,-9.669764638e-01f,-8.639728427e-01f,-5.035383701e-01f,
8.700869679e-01f,-4.928981960e-01f,-4.928981960e-01f,-8.700869679e-01f,-8.700869679e-01f,4.928981960e-01f,4.928981960e-01f,8.700869679e-01f,
7.157308459e-01f,-6.983762383e-01f,-9.999247193e-01f,-1.227153838e-02f,6.983762383e-01f,7.157308459e-01f,1.227153838e-02f,-9.999247193e-01f,
5.000000000e-01f,5.000000000e-01f,5.000000000e-01f,-5.000000000e-01f,5.000000000e-01f,5.000000000e-01f,5.035383701e-01f,-8.639728427e-01f,
9.661899805e-01f,-2.578310966e-01f,5.008853674e-01f,-8.655136228e-01f,-2.578310966e-01f,-9.661899805e-01f,-8.655136228e-01f,-5.008853674e-01f,
8.670462370e-01f,-4.982276559e-01f,-4.982276559e-01f,-8.670462370e-01f,-8.670462370e-01f,4.982276559e-01f,4.982276559e-01f,8.670462370e-01f,
7.092728019e-01f,-7.049340606e-01f,-9.999952912e-01f,-3.067956772e-03f,7.049340606e-01f,7.092728019e-01f,3.067956772e-03f,-9.999952912e-01f,
5.000000000e-01f,5.000000000e-01f,5.000000000e-01f,-5.000000000e-01f,5.000000000e-01f,5.000000000e-01f,4.928981960e-01f,-8.700869679e-01f,
9.653944373e-01f,-2.607941031e-01f,4.982276559e-01f,-8.670462370e-01f,-2.607941031e-01f,-9.653944373e-01f,-8.670462370e-01f,-4.982276559e-01f,
8.639728427e-01f,-5.035383701e-01f,-5.035383701e-01f,-8.639728427e-01f,-8.639728427e-01f,5.035383701e-01f,5.035383701e-01f,8.639728427e-01f,
7.027547359e-01f,-7.114322186e-01f,-9.999811649e-01f,6.135884672e-03f,7.114322186e-01f,7.027547359e-01f,-6.135884672e-03f,-9.999811649e-01f,
5.000000000e-01f,5.000000000e-01f,5.000000000e-01f,-5.000000000e-01f,5.000000000e-01f,5.000000000e-01f,4.821837842e-01f,-8.760700822e-01f,
9.645897746e-01f,-2.637546659e-01f,4.955652654e-01f,-8.685706854e-01f,-2.637546659e-01f,-9.645897746e-01f,-8.685706854e-01f,-4.955652654e-01f,
8.608669639e-01f,-5.088301301e-01f,-5.088301301e-01f,-8.608669639e-01f,-8.608669639e-01f,5.088301301e-01f,5.088301301e-01f,8.608669639e-01f,
6.961771250e-01f,-7.178700566e-01f,-9.998823404e-01f,1.533920597e-02f,7.178700566e-01f,6.961771250e-01f,-1.533920597e-02f,-9.998823404e-01f,
5.000000000e-01f,5.000000000e-01f,5.000000000e-01f,-5.000000000e-01f,5.000000000e-01f,5.000000000e-01f,4.713967443e-01f,-8.819212914e-01f,
9.637760520e-01f,-2.667127550e-01f,4.928981960e-01f,-8.700869679e-01f,-2.667127550e-01f,-9.637760520e-01f,-8.700869679e-01f,-4.928981960e-01f,
8.577286005e-01f,-5.141027570e-01f,-5.141027570e-01f,-8.577286005e-01f,-8.577286005e-01f,5.141027570e-01f,5.141027570e-01f,8.577286005e-01f,
6.895405650e-01f,-7.242470980e-01f,-9.996988177e-01f,2.454122901e-02f,7.242470980e-01f,6.895405650e-01f,-2.454122901e-02f,-9.996988177e-01f,
5.000000000e-01f,5.000000000e-01f,5.000000000e-01f,-5.000000000e-01f,5.000000000e-01f,5.000000000e-01f,4.605387151e-01f,-8.876396418e-01f,
9.629532695e-01f,-2.696683109e-01f,4.902264774e-01f,-8.715950847e-01f,-2.696683109e-01f,-9.629532695e-01f,-8.715950847e-01f,-4.902264774e-01f,
8.545579910e-01f,-5.193560123e-01f,-5.193560123e-01f,-8.545579910e-01f,-8.545579910e-01f,5.193560123e-01f,5.193560123e-01f,8.545579910e-01f,
6.828455329e-01f,-7.305627465e-01f,-9.994305968e-01f,3.374117240e-02f,7.305627465e-01f,6.828455329e-01f,-3.374117240e-02f,-9.994305968e-01f,
5.000000000e-01f,5.000000000e-01f,5.000000000e-01f,-5.000000000e-01f,5.000000000e-01f,5.000000000e-01f,4.496113360e-01f,-8.932242990e-01f,
9.621214271e-01f,-2.726213634e-01f,4.875501692e-01f,-8.730949759e-01f,-2.726213634e-01f,-9.621214271e-01f,-8.730949759e-01f,-4.875501692e-01f,
8.513551950e-01f,-5.245896578e-01f,-5.245896578e-01f,-8.513551950e-01f,-8.513551950e-01f,5.245896578e-01f,5.245896578e-01f,8.513551950e-01f,
6.760926843e-01f,-7.368165851e-01f,-9.990777373e-01f,4.293825850e-02f,7.368165851e-01f,6.760926843e-01f,-4.293825850e-02f,-9.990777373e-01f,
5.000000000e-01f,5.000000000e-01f,5.000000000e-01f,-5.000000000e-01f,5.000000000e-01f,5.000000000e-01f,4.386162460e-01f,-8.986744881e-01f,
9.612804651e-01f,-2.755718231e-01f,4.848692417e-01f,-8.745866418e-01f,-2.755718231e-01f,-9.612804651e-01f,-8.745866418e-01f,-4.848692417e-01f,
8.481203318e-01f,-5.298036337e-01f,-5.298036337e-01f,-8.481203318e-01f,-8.481203318e-01f,5.298036337e-01f,5.298036337e-01f,8.481203318e-01f,
6.692826152e-01f,-7.430079579e-01f,-9.986402392e-01f,5.213170499e-02f,7.430079579e-01f,6.692826152e-01f,-5.213170499e-02f,-9.986402392e-01f,
5.000000000e-01f,5.000000000e-01f,5.000000000e-01f,-5.000000000e-01f,5.000000000e-01f,5.000000000e-01f,4.275550842e-01f,-9.039893150e-01f,
9.604305029e-01f,-2.785196900e-01f,4.821837842e-01f,-8.760700822e-01f,-2.785196900e-01f,-9.604305029e-01f,-8.760700822e-01f,-4.821837842e-01f,
8.448535800e-01f,-5.349976420e-01f,-5.349976420e-01f,-8.448535800e-01f,-8.448535800e-01f,5.349976420e-01f,5.349976420e-01f,8.448535800e-01f,
6.624158025e-01f,-7.491363883e-01f,-9.981181026e-01f,6.132073700e-02f,7.491363883e-01f,6.624158025e-01f,-6.132073700e-02f,-9.981181026e-01f,
5.000000000e-01f,5.000000000e-01f,5.000000000e-01f,-5.000000000e-01f,5.000000000e-01f,5.000000000e-01f,4.164295495e-01f,-9.091680050e-01f,
9.595715404e-01f,-2.814649343e-01f,4.794937670e-01f,-8.775452971e-01f,-2.814649343e-01f,-9.595715404e-01f,-8.775452971e-01f,-4.794937670e-01f,
8.415549994e-01f,-5.401714444e-01f,-5.401714444e-01f,-8.415549994e-01f,-8.415549994e-01f,5.401714444e-01f,5.401714444e-01f,8.415549994e-01f,
6.554928422e-01f,-7.552013993e-01f,-9.975114465e-01f,7.050457597e-02f,7.552013993e-01f,6.554928422e-01f,-7.050457597e-02f,-9.975114465e-01f,
5.000000000e-01f,5.000000000e-01f,5.000000000e-01f,-5.000000000e-01f,5.000000000e-01f,5.000000000e-01f,4.052413106e-01f,-9.142097831e-01f,
9.587034583e-01f,-2.844075263e-01f,4.767992198e-01f,-8.790122271e-01f,-2.844075263e-01f,-9.587034583e-01f,-8.790122271e-01f,-4.767992198e-01f,
8.382247090e-01f,-5.453249812e-01f,-5.453249812e-01f,-8.382247090e-01f,-8.382247090e-01f,5.453249812e-01f,5.453249812e-01f,8.382247090e-01f,
6.485143900e-01f,-7.612023950e-01f,-9.968202710e-01f,7.968243957e-02f,7.612023950e-01f,6.485143900e-01f,-7.968243957e-02f,-9.968202710e-01f,
5.000000000e-01f,5.000000000e-01f,5.000000000e-01f,-5.000000000e-01f,5.000000000e-01f,5.000000000e-01f,3.939920366e-01f,-9.191138744e-01f,
9.578264356e-01f,-2.873474658e-01f,4.741002023e-01f,-8.804708719e-01f,-2.873474658e-01f,-9.578264356e-01f,-8.804708719e-01f,-4.741002023e-01f,
8.348628879e-01f,-5.504579544e-01f,-5.504579544e-01f,-8.348628879e-01f,-8.348628879e-01f,5.504579544e-01f,5.504579544e-01f,8.348628879e-01f,
6.414810419e-01f,-7.671388984e-01f,-9.960446954e-01f,8.885355294e-02f,7.671388984e-01f,6.414810419e-01f,-8.885355294e-02f,-9.960446954e-01f,
5.000000000e-01f,5.000000000e-01f,5.000000000e-01f,-5.000000000e-01f,5.000000000e-01f,5.000000000e-01f,3.826834261e-01f,-9.238795042e-01f,
9.569403529e-01f,-2.902846634e-01f,4.713967443e-01f,-8.819212914e-01f,-2.902846634e-01f,-9.569403529e-01f,-8.819212914e-01f,-4.713967443e-01f,
8.314695954e-01f,-5.555702448e-01f,-5.555702448e-01f,-8.314695954e-01f,-8.314695954e-01f,5.555702448e-01f,5.555702448e-01f,8.314695954e-01f,
6.343932748e-01f,-7.730104327e-01f,-9.951847196e-01f,9.801714122e-02f,7.730104327e-01f,6.343932748e-01f,-9.801714122e-02f,-9.951847196e-01f,
5.000000000e-01f,5.000000000e-01f,5.000000000e-01f,-5.000000000e-01f,5.000000000e-01f,5.000000000e-01f,3.713172078e-01f,-9.285060763e-01f,
9.560452700e-01f,-2.932191491e-01f,4.686888158e-01f,-8.833633661e-01f,-2.932191491e-01f,-9.560452700e-01f,-8.833633661e-01f,-4.686888158e-01f,
8.280450702e-01f,-5.606615543e-01f,-5.606615543e-01f,-8.280450702e-01f,-8.280450702e-01f,5.606615543e-01f,5.606615543e-01f,8.280450702e-01f,
6.272518039e-01f,-7.788165212e-01f,-9.942404628e-01f,1.071724221e-01f,7.788165212e-01f,6.272518039e-01f,-1.071724221e-01f,-9.942404628e-01f,
5.000000000e-01f,5.000000000e-01f,5.000000000e-01f,-5.000000000e-01f,5.000000000e-01f,5.000000000e-01f,3.598950505e-01f,-9.329928160e-01f,
9.551411867e-01f,-2.961508930e-01f,4.659765065e-01f,-8.847970963e-01f,-2.961508930e-01f,-9.551411867e-01f,-8.847970963e-01f,-4.659765065e-01f,
8.245893121e-01f,-5.657318234e-01f,-5.657318234e-01f,-8.245893121e-01f,-8.245893121e-01f,5.657318234e-01f,5.657318234e-01f,8.245893121e-01f,
6.200572252e-01f,-7.845565677e-01f,-9.932119250e-01f,1.163186282e-01f,7.845565677e-01f,6.200572252e-01f,-1.163186282e-01f,-9.932119250e-01f,
5.000000000e-01f,5.000000000e-01f,5.000000000e-01f,-5.000000000e-01f,5.000000000e-01f,5.000000000e-01f,3.484186828e-01f,-9.373390079e-01f,
9.542281032e-01f,-2.990798354e-01f,4.632597864e-01f,-8.862225413e-01f,-2.990798354e-01f,-9.542281032e-01f,-8.862225413e-01f,-4.632597864e-01f,
8.211025000e-01f,-5.707807541e-01f,-5.707807541e-01f,-8.211025000e-01f,-8.211025000e-01f,5.707807541e-01f,5.707807541e-01f,8.211025000e-01f,
6.128100753e-01f,-7.902302146e-01f,-9.920992851e-01f,1.254549772e-01f,7.902302146e-01f,6.128100753e-01f,-1.254549772e-01f,-9.920992851e-01f,
5.000000000e-01f,5.000000000e-01f,5.000000000e-01f,-5.000000000e-01f,5.000000000e-01f,5.000000000e-01f,3.368898630e-01f,-9.415440559e-01f,
9.533060193e-01f,-3.020059466e-01f,4.605387151e-01f,-8.876396418e-01f,-3.020059466e-01f,-9.533060193e-01f,-8.876396418e-01f,-4.605387151e-01f,
8.175848126e-01f,-5.758081675e-01f,-5.758081675e-01f,-8.175848126e-01f,-8.175848126e-01f,5.758081675e-01f,5.758081675e-01f,8.175848126e-01f,
6.055110693e-01f,-7.958369255e-01f,-9.909026623e-01f,1.345807016e-01f,7.958369255e-01f,6.055110693e-01f,-1.345807016e-01f,-9.909026623e-01f,
5.000000000e-01f,5.000000000e-01f,5.000000000e-01f,-5.000000000e-01f,5.000000000e-01f,5.000000000e-01f,3.253102899e-01f,-9.456073046e-01f,
9.523749948e-01f,-3.049292266e-01f,4.578132927e-01f,-8.890483379e-01f,-3.049292266e-01f,-9.523749948e-01f,-8.890483379e-01f,-4.578132927e-01f,
8.140363097e-01f,-5.808139443e-01f,-5.808139443e-01f,-8.140363097e-01f,-8.140363097e-01f,5.808139443e-01f,5.808139443e-01f,8.140363097e-01f,
5.981606841e-01f,-8.013761640e-01f,-9.896219969e-01f,1.436950266e-01f,8.013761640e-01f,5.981606841e-01f,-1.436950266e-01f,-9.896219969e-01f,
5.000000000e-01f,5.000000000e-01f,5.000000000e-01f,-5.000000000e-01f,5.000000000e-01f,5.000000000e-01f,3.136817515e-01f,-9.495281577e-01f,
9.514350295e-01f,-3.078496456e-01f,4.550835788e-01f,-8.904487491e-01f,-3.078496456e-01f,-9.514350295e-01f,-8.904487491e-01f,-4.550835788e-01f,
8.104571700e-01f,-5.857978463e-01f,-5.857978463e-01f,-8.104571700e-01f,-8.104571700e-01f,5.857978463e-01f,5.857978463e-01f,8.104571700e-01f,
5.907596946e-01f,-8.068475723e-01f,-9.882575870e-01f,1.527971923e-01f,8.068475723e-01f,5.907596946e-01f,-1.527971923e-01f,-9.882575870e-01f,
5.000000000e-01f,5.000000000e-01f,5.000000000e-01f,-5.000000000e-01f,5.000000000e-01f,5.000000000e-01f,3.020059466e-01f,-9.533060193e-01f,
9.504860640e-01f,-3.107671440e-01f,4.523495734e-01f,-8.918406963e-01f,-3.107671440e-01f,-9.504860640e-01f,-8.918406963e-01f,-4.523495734e-01f,
8.068475723e-01f,-5.907596946e-01f,-5.907596946e-01f,-8.068475723e-01f,-8.068475723e-01f,5.907596946e-01f,5.907596946e-01f,8.068475723e-01f,
5.833086371e-01f,-8.122506142e-01f,-9.868093729e-01f,1.618863940e-01f,8.122506142e-01f,5.833086371e-01f,-1.618863940e-01f,-9.868093729e-01f,
5.000000000e-01f,5.000000000e-01f,5.000000000e-01f,-5.000000000e-01f,5.000000000e-01f,5.000000000e-01f,2.902846634e-01f,-9.569403529e-01f,
9.495281577e-01f,-3.136817515e-01f,4.496113360e-01f,-8.932242990e-01f,-3.136817515e-01f,-9.495281577e-01f,-8.932242990e-01f,-4.496113360e-01f,
8.032075167e-01f,-5.956993103e-01f,-5.956993103e-01f,-8.032075167e-01f,-8.032075167e-01f,5.956993103e-01f,5.956993103e-01f,8.032075167e-01f,
5.758081675e-01f,-8.175848126e-01f,-9.852776527e-01f,1.709618866e-01f,8.175848126e-01f,5.758081675e-01f,-1.709618866e-01f,-9.852776527e-01f,
5.000000000e-01f,5.000000000e-01f,5.000000000e-01f,-5.000000000e-01f,5.000000000e-01f,5.000000000e-01f,2.785196900e-01f,-9.604305029e-01f,
9.485613704e-01f,-3.165933788e-01f,4.468688369e-01f,-8.945994973e-01f,-3.165933788e-01f,-9.485613704e-01f,-8.945994973e-01f,-4.468688369e-01f,
7.995372415e-01f,-6.006164551e-01f,-6.006164551e-01f,-7.995372415e-01f,-7.995372415e-01f,6.006164551e-01f,6.006164551e-01f,7.995372415e-01f,
5.682589412e-01f,-8.228498101e-01f,-9.836624265e-01f,1.800228953e-01f,8.228498101e-01f,5.682589412e-01f,-1.800228953e-01f,-9.836624265e-01f,
5.000000000e-01f,5.000000000e-01f,5.000000000e-01f,-5.000000000e-01f,5.000000000e-01f,5.000000000e-01f,2.667127550e-01f,-9.637760520e-01f,
9.475855827e-01f,-3.195020258e-01f,4.441221356e-01f,-8.959662318e-01f,-3.195020258e-01f,-9.475855827e-01f,-8.959662318e-01f,-4.441221356e-01f,
7.958369255e-01f,-6.055110693e-01f,-6.055110693e-01f,-7.958369255e-01f,-7.958369255e-01f,6.055110693e-01f,6.055110693e-01f,7.958369255e-01f,
5.606615543e-01f,-8.280450702e-01f,-9.819638729e-01f,1.890686601e-01f,8.280450702e-01f,5.606615543e-01f,-1.890686601e-01f,-9.819638729e-01f,
5.000000000e-01f,5.000000000e-01f,5.000000000e-01f,-5.000000000e-01f,5.000000000e-01f,5.000000000e-01f,2.548656464e-01f,-9.669764638e-01f,
9.466009140e-01f,-3.224076927e-01f,4.413712621e-01f,-8.973245621e-01f,-3.224076927e-01f,-9.466009140e-01f,-8.973245621e-01f,-4.413712621e-01f,
7.921065688e-01f,-6.103827953e-01f,-6.103827953e-01f,-7.921065688e-01f,-7.921065688e-01f,6.103827953e-01f,6.103827953e-01f,7.921065688e-01f,
5.530167222e-01f,-8.331701756e-01f,-9.801821113e-01f,1.980984062e-01f,8.331701756e-01f,5.530167222e-01f,-1.980984062e-01f,-9.801821113e-01f,
5.000000000e-01f,5.000000000e-01f,5.000000000e-01f,-5.000000000e-01f,5.000000000e-01f,5.000000000e-01f,2.429801822e-01f,-9.700312614e-01f,
9.456073046e-01f,-3.253102899e-01f,4.386162460e-01f,-8.986744881e-01f,-3.253102899e-01f,-9.456073046e-01f,-8.986744881e-01f,-4.386162460e-01f,
7.883464098e-01f,-6.152315736e-01f,-6.152315736e-01f,-7.883464098e-01f,-7.883464098e-01f,6.152315736e-01f,6.152315736e-01f,7.883464098e-01f,
5.453249812e-01f,-8.382247090e-01f,-9.783173800e-01f,2.071113735e-01f,8.382247090e-01f,5.453249812e-01f,-2.071113735e-01f,-9.783173800e-01f,
5.000000000e-01f,5.000000000e-01f,5.000000000e-01f,-5.000000000e-01f,5.000000000e-01f,5.000000000e-01f,2.310581058e-01f,-9.729399681e-01f,
9.446048141e-01f,-3.282098472e-01f,4.358570874e-01f,-9.000158906e-01f,-3.282098472e-01f,-9.446048141e-01f,-9.000158906e-01f,-4.358570874e-01f,
7.845565677e-01f,-6.200572252e-01f,-6.200572252e-01f,-7.845565677e-01f,-7.845565677e-01f,6.200572252e-01f,6.200572252e-01f,7.845565677e-01f,
5.375870466e-01f,-8.432082534e-01f,-9.763697386e-01f,2.161068022e-01f,8.432082534e-01f,5.375870466e-01f,-2.161068022e-01f,-9.763697386e-01f,
5.000000000e-01f,5.000000000e-01f,5.000000000e-01f,-5.000000000e-01f,5.000000000e-01f,5.000000000e-01f,2.191012353e-01f,-9.757021070e-01f,
9.435934424e-01f,-3.311063051e-01f,4.330938160e-01f,-9.013488293e-01f,-3.311063051e-01f,-9.435934424e-01f,-9.013488293e-01f,-4.330938160e-01f,
7.807372212e-01f,-6.248595119e-01f,-6.248595119e-01f,-7.807372212e-01f,-7.807372212e-01f,6.248595119e-01f,6.248595119e-01f,7.807372212e-01f,
5.298036337e-01f,-8.481203318e-01f,-9.743393660e-01f,2.250839174e-01f,8.481203318e-01f,5.298036337e-01f,-2.250839174e-01f,-9.743393660e-01f,
5.000000000e-01f,5.000000000e-01f,5.000000000e-01f,-5.000000000e-01f,5.000000000e-01f,5.000000000e-01f,2.071113735e-01f,-9.783173800e-01f,
9.425731897e-01f,-3.339996636e-01f,4.303264916e-01f,-9.026733041e-01f,-3.339996636e-01f,-9.425731897e-01f,-9.026733041e-01f,-4.303264916e-01f,
7.768884897e-01f,-6.296382546e-01f,-6.296382546e-01f,-7.768884897e-01f,-7.768884897e-01f,6.296382546e-01f,6.296382546e-01f,7.768884897e-01f,
5.219752789e-01f,-8.529605865e-01f,-9.722265005e-01f,2.340419590e-01f,8.529605865e-01f,5.219752789e-01f,-2.340419590e-01f,-9.722265005e-01f,
5.000000000e-01f,5.000000000e-01f,5.000000000e-01f,-5.000000000e-01f,5.000000000e-01f,5.000000000e-01f,1.950903237e-01f,-9.807852507e-01f,
9.415440559e-01f,-3.368898630e-01f,4.275550842e-01f,-9.039893150e-01f,-3.368898630e-01f,-9.415440559e-01f,-9.039893150e-01f,-4.275550842e-01f,
7.730104327e-01f,-6.343932748e-01f,-6.343932748e-01f,-7.730104327e-01f,-7.730104327e-01f,6.343932748e-01f,6.343932748e-01f,7.730104327e-01f,
5.141027570e-01f,-8.577286005e-01f,-9.700312614e-01f,2.429801822e-01f,8.577286005e-01f,5.141027570e-01f,-2.429801822e-01f,-9.700312614e-01f,
5.000000000e-01f,5.000000000e-01f,5.000000000e-01f,-5.000000000e-01f,5.000000000e-01f,5.000000000e-01f,1.830398887e-01f,-9.831054807e-01f,
9.405060410e-01f,-3.397768736e-01f,4.247796834e-01f,-9.052967429e-01f,-3.397768736e-01f,-9.405060410e-01f,-9.052967429e-01f,-4.247796834e-01f,
7.691033483e-01f,-6.391244531e-01f,-6.391244531e-01f,-7.691033483e-01f,-7.691033483e-01f,6.391244531e-01f,6.391244531e-01f,7.691033483e-01f,
5.061866641e-01f,-8.624239564e-01f,-9.677538276e-01f,2.518978119e-01f,8.624239564e-01f,5.061866641e-01f,-2.518978119e-01f,-9.677538276e-01f,
5.000000000e-01f,5.000000000e-01f,5.000000000e-01f,-5.000000000e-01f,5.000000000e-01f,5.000000000e-01f,1.709618866e-01f,-9.852776527e-01f,
9.394592047e-01f,-3.426607251e-01f,4.220002592e-01f,-9.065957069e-01f,-3.426607251e-01f,-9.394592047e-01f,-9.065957069e-01f,-4.220002592e-01f,
7.651672363e-01f,-6.438315511e-01f,-6.438315511e-01f,-7.651672363e-01f,-7.651672363e-01f,6.438315511e-01f,6.438315511e-01f,7.651672363e-01f,
4.982276559e-01f,-8.670462370e-01f,-9.653944373e-01f,2.607941031e-01f,8.670462370e-01f,4.982276559e-01f,-2.607941031e-01f,-9.653944373e-01f,
5.000000000e-01f,5.000000000e-01f,5.000000000e-01f,-5.000000000e-01f,5.000000000e-01f,5.000000000e-01f,1.588581502e-01f,-9.873014092e-01f,
9.384035468e-01f,-3.455413282e-01f,4.192169011e-01f,-9.078860879e-01f,-3.455413282e-01f,-9.384035468e-01f,-9.078860879e-01f,-4.192169011e-01f,
7.612023950e-01f,-6.485143900e-01f,-6.485143900e-01f,-7.612023950e-01f,-7.612023950e-01f,6.485143900e-01f,6.485143900e-01f,7.612023950e-01f,
4.902264774e-01f,-8.715950847e-01f,-9.629532695e-01f,2.696683109e-01f,8.715950847e-01f,4.902264774e-01f,-2.696683109e-01f,-9.629532695e-01f,
5.000000000e-01f,5.000000000e-01f,5.000000000e-01f,-5.000000000e-01f,5.000000000e-01f,5.000000000e-01f,1.467304677e-01f,-9.891765118e-01f,
9.373390079e-01f,-3.484186828e-01f,4.164295495e-01f,-9.091680050e-01f,-3.484186828e-01f,-9.373390079e-01f,-9.091680050e-01f,-4.164295495e-01f,
7.572088242e-01f,-6.531728506e-01f,-6.531728506e-01f,-7.572088242e-01f,-7.572088242e-01f,6.531728506e-01f,6.531728506e-01f,7.572088242e-01f,
4.821837842e-01f,-8.760700822e-01f,-9.604305029e-01f,2.785196900e-01f,8.760700822e-01f,4.821837842e-01f,-2.785196900e-01f,-9.604305029e-01f,
5.000000000e-01f,5.000000000e-01f,5.000000000e-01f,-5.000000000e-01f,5.000000000e-01f,5.000000000e-01f,1.345807016e-01f,-9.909026623e-01f,
9.362656474e-01f,-3.512927592e-01f,4.136383235e-01f,-9.104412794e-01f,-3.512927592e-01f,-9.362656474e-01f,-9.104412794e-01f,-4.136383235e-01f,
7.531868219e-01f,-6.578066945e-01f,-6.578066945e-01f,-7.531868219e-01f,-7.531868219e-01f,6.578066945e-01f,6.578066945e-01f,7.531868219e-01f,
4.741002023e-01f,-8.804708719e-01f,-9.578264356e-01f,2.873474658e-01f,8.804708719e-01f,4.741002023e-01f,-2.873474658e-01f,-9.578264356e-01f,
5.000000000e-01f,5.000000000e-01f,5.000000000e-01f,-5.000000000e-01f,5.000000000e-01f,5.000000000e-01f,1.224106774e-01f,-9.924795628e-01f,
9.351835251e-01f,-3.541635275e-01f,4.108431637e-01f,-9.117060304e-01f,-3.541635275e-01f,-9.351835251e-01f,-9.117060304e-01f,-4.108431637e-01f,
7.491363883e-01f,-6.624158025e-01f,-6.624158025e-01f,-7.491363883e-01f,-7.491363883e-01f,6.624158025e-01f,6.624158025e-01f,7.491363883e-01f,
4.659765065e-01f,-8.847970963e-01f,-9.551411867e-01f,2.961508930e-01f,8.847970963e-01f,4.659765065e-01f,-2.961508930e-01f,-9.551411867e-01f,
5.000000000e-01f,5.000000000e-01f,5.000000000e-01f,-5.000000000e-01f,5.000000000e-01f,5.000000000e-01f,1.102222055e-01f,-9.939069748e-01f,
9.340925217e-01f,-3.570309579e-01f,4.080441594e-01f,-9.129621983e-01f,-3.570309579e-01f,-9.340925217e-01f,-9.129621983e-01f,-4.080441594e-01f,
7.450577617e-01f,-6.669999361e-01f,-6.669999361e-01f,-7.450577617e-01f,-7.450577617e-01f,6.669999361e-01f,6.669999361e-01f,7.450577617e-01f,
4.578132927e-01f,-8.890483379e-01f,-9.523749948e-01f,3.049292266e-01f,8.890483379e-01f,4.578132927e-01f,-3.049292266e-01f,-9.523749948e-01f,
5.000000000e-01f,5.000000000e-01f,5.000000000e-01f,-5.000000000e-01f,5.000000000e-01f,5.000000000e-01f,9.801714122e-02f,-9.951847196e-01f,
9.329928160e-01f,-3.598950505e-01f,4.052413106e-01f,-9.142097831e-01f,-3.598950505e-01f,-9.329928160e-01f,-9.142097831e-01f,-4.052413106e-01f,
7.409511209e-01f,-6.715589762e-01f,-6.715589762e-01f,-7.409511209e-01f,-7.409511209e-01f,6.715589762e-01f,6.715589762e-01f,7.409511209e-01f,
4.496113360e-01f,-8.932242990e-01f,-9.495281577e-01f,3.136817515e-01f,8.932242990e-01f,4.496113360e-01f,-3.136817515e-01f,-9.495281577e-01f,
5.000000000e-01f,5.000000000e-01f,5.000000000e-01f,-5.000000000e-01f,5.000000000e-01f,5.000000000e-01f,8.579730988e-02f,-9.963126183e-01f,
9.318842888e-01f,-3.627557158e-01f,4.024346471e-01f,-9.154487252e-01f,-3.627557158e-01f,-9.318842888e-01f,-9.154487252e-01f,-4.024346471e-01f,
7.368165851e-01f,-6.760926843e-01f,-6.760926843e-01f,-7.368165851e-01f,-7.368165851e-01f,6.760926843e-01f,6.760926843e-01f,7.368165851e-01f,
4.413712621e-01f,-8.973245621e-01f,-9.466009140e-01f,3.224076927e-01f,8.973245621e-01f,4.413712621e-01f,-3.224076927e-01f,-9.466009140e-01f,
5.000000000e-01f,5.000000000e-01f,5.000000000e-01f,-5.000000000e-01f,5.000000000e-01f,5.000000000e-01f,7.356456667e-02f,-9.972904325e-01f,
9.307669401e-01f,-3.656129837e-01f,3.996241987e-01f,-9.166790843e-01f,-3.656129837e-01f,-9.307669401e-01f,-9.166790843e-01f,-3.996241987e-01f,
7.326542735e-01f,-6.806010008e-01f,-6.806010008e-01f,-7.326542735e-01f,-7.326542735e-01f,6.806010008e-01f,6.806010008e-01f,7.326542735e-01f,
4.330938160e-01f,-9.013488293e-01f,-9.435934424e-01f,3.311063051e-01f,9.013488293e-01f,4.330938160e-01f,-3.311063051e-01f,-9.435934424e-01f,
5.000000000e-01f,5.000000000e-01f,5.000000000e-01f,-5.000000000e-01f,5.000000000e-01f,5.000000000e-01f,6.132073700e-02f,-9.981181026e-01f,
9.296408892e-01f,-3.684668243e-01f,3.968099952e-01f,-9.179008007e-01f,-3.684668243e-01f,-9.296408892e-01f,-9.179008007e-01f,-3.968099952e-01f,
7.284643650e-01f,-6.850836873e-01f,-6.850836873e-01f,-7.284643650e-01f,-7.284643650e-01f,6.850836873e-01f,6.850836873e-01f,7.284643650e-01f,
4.247796834e-01f,-9.052967429e-01f,-9.405060410e-01f,3.397768736e-01f,9.052967429e-01f,4.247796834e-01f,-3.397768736e-01f,-9.405060410e-01f,
5.000000000e-01f,5.000000000e-01f,5.000000000e-01f,-5.000000000e-01f,5.000000000e-01f,5.000000000e-01f,4.906767607e-02f,-9.987954497e-01f,
9.285060763e-01f,-3.713172078e-01f,3.939920366e-01f,-9.191138744e-01f,-3.713172078e-01f,-9.285060763e-01f,-9.191138744e-01f,-3.939920366e-01f,
7.242470980e-01f,-6.895405650e-01f,-6.895405650e-01f,-7.242470980e-01f,-7.242470980e-01f,6.895405650e-01f,6.895405650e-01f,7.242470980e-01f,
4.164295495e-01f,-9.091680050e-01f,-9.373390079e-01f,3.484186828e-01f,9.091680050e-01f,4.164295495e-01f,-3.484186828e-01f,-9.373390079e-01f,
5.000000000e-01f,5.000000000e-01f,5.000000000e-01f,-5.000000000e-01f,5.000000000e-01f,5.000000000e-01f,3.680722415e-02f,-9.993223548e-01f,
9.273625016e-01f,-3.741640747e-01f,3.911703825e-01f,-9.203183055e-01f,-3.741640747e-01f,-9.273625016e-01f,-9.203183055e-01f,-3.911703825e-01f,
7.200025320e-01f,-6.939714551e-01f,-6.939714551e-01f,-7.200025320e-01f,-7.200025320e-01f,6.939714551e-01f,6.939714551e-01f,7.200025320e-01f,
4.080441594e-01f,-9.129621983e-01f,-9.340925217e-01f,3.570309579e-01f,9.129621983e-01f,4.080441594e-01f,-3.570309579e-01f,-9.340925217e-01f,
5.000000000e-01f,5.000000000e-01f,5.000000000e-01f,-5.000000000e-01f,5.000000000e-01f,5.000000000e-01f,2.454122901e-02f,-9.996988177e-01f,
9.262102246e-01f,-3.770074248e-01f,3.883450329e-01f,-9.215140343e-01f,-3.770074248e-01f,-9.262102246e-01f,-9.215140343e-01f,-3.883450329e-01f,
7.157308459e-01f,-6.983762383e-01f,-6.983762383e-01f,-7.157308459e-01f,-7.157308459e-01f,6.983762383e-01f,6.983762383e-01f,7.157308459e-01f,
3.996241987e-01f,-9.166790843e-01f,-9.307669401e-01f,3.656129837e-01f,9.166790843e-01f,3.996241987e-01f,-3.656129837e-01f,-9.307669401e-01f,
5.000000000e-01f,5.000000000e-01f,5.000000000e-01f,-5.000000000e-01f,5.000000000e-01f,5.000000000e-01f,1.227153838e-02f,-9.999247193e-01f,
9.250492454e-01f,-3.798471987e-01f,3.855160475e-01f,-9.227011204e-01f,-3.798471987e-01f,-9.250492454e-01f,-9.227011204e-01f,-3.855160475e-01f,
7.114322186e-01f,-7.027547359e-01f,-7.027547359e-01f,-7.114322186e-01f,-7.114322186e-01f,7.027547359e-01f,7.027547359e-01f,7.114322186e-01f,
3.911703825e-01f,-9.203183055e-01f,-9.273625016e-01f,3.741640747e-01f,9.203183055e-01f,3.911703825e-01f,-3.741640747e-01f,-9.273625016e-01f,
5.000000000e-01f,5.000000000e-01f,5.000000000e-01f,-5.000000000e-01f,5.000000000e-01f,5.000000000e-01f,0.000000000e+00f,-1.000000000e+00f,
9.238795042e-01f,-3.826834261e-01f,3.826834261e-01f,-9.238795042e-01f,-3.826834261e-01f,-9.238795042e-01f,-9.238795042e-01f,-3.826834261e-01f,
7.071067691e-01f,-7.071067691e-01f,-7.071067691e-01f,-7.071067691e-01f,-7.071067691e-01f,7.071067691e-01f,7.071067691e-01f,7.071067691e-01f,
3.826834261e-01f,-9.238795042e-01f,-9.238795042e-01f,3.826834261e-01f,9.238795042e-01f,3.826834261e-01f,-3.826834261e-01f,-9.238795042e-01f,
5.000000000e-01f,5.000000000e-01f,5.000000000e-01f,-5.000000000e-01f,5.000000000e-01f,5.000000000e-01f,-1.227153838e-02f,-9.999247193e-01f,
9.227011204e-01f,-3.855160475e-01f,3.798471987e-01f,-9.250492454e-01f,-3.855160475e-01f,-9.227011204e-01f,-9.250492454e-01f,-3.798471987e-01f,
7.027547359e-01f,-7.114322186e-01f,-7.114322186e-01f,-7.027547359e-01f,-7.027547359e-01f,7.114322186e-01f,7.114322186e-01f,7.027547359e-01f,
3.741640747e-01f,-9.273625016e-01f,-9.203183055e-01f,3.911703825e-01f,9.273625016e-01f,3.741640747e-01f,-3.911703825e-01f,-9.203183055e-01f,
5.000000000e-01f,5.000000000e-01f,5.000000000e-01f,-5.000000000e-01f,5.000000000e-01f,5.000000000e-01f,-2.454122901e-02f,-9.996988177e-01f,
9.215140343e-01f,-3.883450329e-01f,3.770074248e-01f,-9.262102246e-01f,-3.883450329e-01f,-9.215140343e-01f,-9.262102246e-01f,-3.770074248e-01f,
6.983762383e-01f,-7.157308459e-01f,-7.157308459e-01f,-6.983762383e-01f,-6.983762383e-01f,7.157308459e-01f,7.157308459e-01f,6.983762383e-01f,
3.656129837e-01f,-9.307669401e-01f,-9.166790843e-01f,3.996241987e-01f,9.307669401e-01f,3.656129837e-01f,-3.996241987e-01f,-9.166790843e-01f,
5.000000000e-01f,5.000000000e-01f,5.000000000e-01f,-5.000000000e-01f,5.000000000e-01f,5.000000000e-01f,-3.680722415e-02f,-9.993223548e-01f,
9.203183055e-01f,-3.911703825e-01f,3.741640747e-01f,-9.273625016e-01f,-3.911703825e-01f,-9.203183055e-01f,-9.273625016e-01f,-3.741640747e-01f,
6.939714551e-01f,-7.200025320e-01f,-7.200025320e-01f,-6.939714551e-01f,-6.939714551e-01f,7.200025320e-01f,7.200025320e-01f,6.939714551e-01f,
3.570309579e-01f,-9.340925217e-01f,-9.129621983e-01f,4.080441594e-01f,9.340925217e-01f,3.570309579e-01f,-4.080441594e-01f,-9.129621983e-01f,
5.000000000e-01f,5.000000000e-01f,5.000000000e-01f,-5.000000000e-01f,5.000000000e-01f,5.000000000e-01f,-4.906767607e-02f,-9.987954497e-01f,
9.191138744e-01f,-3.939920366e-01f,3.713172078e-01f,-9.285060763e-01f,-3.939920366e-01f,-9.191138744e-01f,-9.285060763e-01f,-3.713172078e-01f,
6.895405650e-01f,-7.242470980e-01f,-7.242470980e-01f,-6.895405650e-01f,-6.895405650e-01f,7.242470980e-01f,7.242470980e-01f,6.895405650e-01f,
3.484186828e-01f,-9.373390079e-01f,-9.091680050e-01f,4.164295495e-01f,9.373390079e-01f,3.484186828e-01f,-4.164295495e-01f,-9.091680050e-01f,
5.000000000e-01f,5.000000000e-01f,5.000000000e-01f,-5.000000000e-01f,5.000000000e-01f,5.000000000e-01f,-6.132073700e-02f,-9.981181026e-01f,
9.179008007e-01f,-3.968099952e-01f,3.684668243e-01f,-9.296408892e-01f,-3.968099952e-01f,-9.179008007e-01f,-9.296408892e-01f,-3.684668243e-01f,
6.850836873e-01f,-7.284643650e-01f,-7.284643650e-01f,-6.850836873e-01f,-6.850836873e-01f,7.284643650e-01f,7.284643650e-01f,6.850836873e-01f,
3.397768736e-01f,-9.405060410e-01f,-9.052967429e-01f,4.247796834e-01f,9.405060410e-01f,3.397768736e-01f,-4.247796834e-01f,-9.052967429e-01f,
5.000000000e-01f,5.000000000e-01f,5.000000000e-01f,-5.000000000e-01f,5.000000000e-01f,5.000000000e-01f,-7.356456667e-02f,-9.972904325e-01f,
9.166790843e-01f,-3.996241987e-01f,3.656129837e-01f,-9.307669401e-01f,-3.996241987e-01f,-9.166790843e-01f,-9.307669401e-01f,-3.656129837e-01f,
6.806010008e-01f,-7.326542735e-01f,-7.326542735e-01f,-6.806010008e-01f,-6.806010008e-01f,7.326542735e-01f,7.326542735e-01f,6.806010008e-01f,
3.311063051e-01f,-9.435934424e-01f,-9.013488293e-01f,4.330938160e-01f,9.435934424e-01f,3.311063051e-01f,-4.330938160e-01f,-9.013488293e-01f,
5.000000000e-01f,5.000000000e-01f,5.000000000e-01f,-5.000000000e-01f,5.000000000e-01f,5.000000000e-01f,-8.579730988e-02f,-9.963126183e-01f,
9.154487252e-01f,-4.024346471e-01f,3.627557158e-01f,-9.318842888e-01f,-4.024346471e-01f,-9.154487252e-01f,-9.318842888e-01f,-3.627557158e-01f,
6.760926843e-01f,-7.368165851e-01f,-7.368165851e-01f,-6.760926843e-01f,-6.760926843e-01f,7.368165851e-01f,7.368165851e-01f,6.760926843e-01f,
3.224076927e-01f,-9.466009140e-01f,-8.973245621e-01f,4.413712621e-01f,9.466009140e-01f,3.224076927e-01f,-4.413712621e-01f,-8.973245621e-01f,
5.000000000e-01f,5.000000000e-01f,5.000000000e-01f,-5.000000000e-01f,5.000000000e-01f,5.000000000e-01f,-9.801714122e-02f,-9.951847196e-01f,
9.142097831e-01f,-4.052413106e-01f,3.598950505e-01f,-9.329928160e-01f,-4.052413106e-01f,-9.142097831e-01f,-9.329928160e-01f,-3.598950505e-01f,
6.715589762e-01f,-7.409511209e-01f,-7.409511209e-01f,-6.715589762e-01f,-6.715589762e-01f,7.409511209e-01f,7.409511209e-01f,6.715589762e-01f,
3.136817515e-01f,-9.495281577e-01f,-8.932242990e-01f,4.496113360e-01f,9.495281577e-01f,3.136817515e-01f,-4.496113360e-01f,-8.932242990e-01f,
5.000000000e-01f,5.000000000e-01f,5.000000000e-01f,-5.000000000e-01f,5.000000000e-01f,5.000000000e-01f,-1.102222055e-01f,-9.939069748e-01f,
9.129621983e-01f,-4.080441594e-01f,3.570309579e-01f,-9.340925217e-01f,-4.080441594e-01f,-9.129621983e-01f,-9.340925217e-01f,-3.570309579e-01f,
6.669999361e-01f,-7.450577617e-01f,-7.450577617e-01f,-6.669999361e-01f,-6.669999361e-01f,7.450577617e-01f,7.450577617e-01f,6.669999361e-01f,
3.049292266e-01f,-9.523749948e-01f,-8.890483379e-01f,4.578132927e-01f,9.523749948e-01f,3.049292266e-01f,-4.578132927e-01f,-8.890483379e-01f,
5.000000000e-01f,5.000000000e-01f,5.000000000e-01f,-5.000000000e-01f,5.000000000e-01f,5.000000000e-01f,-1.224106774e-01f,-9.924795628e-01f,
9.117060304e-01f,-4.108431637e-01f,3.541635275e-01f,-9.351835251e-01f,-4.108431637e-01f,-9.117060304e-01f,-9.351835251e-01f,-3.541635275e-01f,
6.624158025e-01f,-7.491363883e-01f,-7.491363883e-01f,-6.624158025e-01f,-6.624158025e-01f,7.491363883e-01f,7.491363883e-01f,6.624158025e-01f,
2.961508930e-01f,-9.551411867e-01f,-8.847970963e-01f,4.659765065e-01f,9.551411867e-01f,2.961508930e-01f,-4.659765065e-01f,-8.847970963e-01f,
5.000000000e-01f,5.000000000e-01f,5.000000000e-01f,-5.000000000e-01f,5.000000000e-01f,5.000000000e-01f,-1.345807016e-01f,-9.909026623e-01f,
9.104412794e-01f,-4.136383235e-01f,3.512927592e-01f,-9.362656474e-01f,-4.136383235e-01f,-9.104412794e-01f,-9.362656474e-01f,-3.512927592e-01f,
6.578066945e-01f,-7.531868219e-01f,-7.531868219e-01f,-6.578066945e-01f,-6.578066945e-01f,7.531868219e-01f,7.531868219e-01f,6.578066945e-01f,
2.873474658e-01f,-9.578264356e-01f,-8.804708719e-01f,4.741002023e-01f,9.578264356e-01f,2.873474658e-01f,-4.741002023e-01f,-8.804708719e-01f,
5.000000000e-01f,5.000000000e-01f,5.000000000e-01f,-5.000000000e-01f,5.000000000e-01f,5.000000000e-01f,-1.467304677e-01f,-9.891765118e-01f,
9.091680050e-01f,-4.164295495e-01f,3.484186828e-01f,-9.373390079e-01f,-4.164295495e-01f,-9.091680050e-01f,-9.373390079e-01f,-3.484186828e-01f,
6.531728506e-01f,-7.572088242e-01f,-7.572088242e-01f,-6.531728506e-01f,-6.531728506e-01f,7.572088242e-01f,7.572088242e-01f,6.531728506e-01f,
2.785196900e-01f,-9.604305029e-01f,-8.760700822e-01f,4.821837842e-01f,9.604305029e-01f,2.785196900e-01f,-4.821837842e-01f,-8.760700822e-01f,
5.000000000e-01f,5.000000000e-01f,5.000000000e-01f,-5.000000000e-01f,5.000000000e-01f,5.000000000e-01f,-1.588581502e-01f,-9.873014092e-01f,
9.078860879e-01f,-4.192169011e-01f,3.455413282e-01f,-9.384035468e-01f,-4.192169011e-01f,-9.078860879e-01f,-9.384035468e-01f,-3.455413282e-01f,
6.485143900e-01f,-7.612023950e-01f,-7.612023950e-01f,-6.485143900e-01f,-6.485143900e-01f,7.612023950e-01f,7.612023950e-01f,6.485143900e-01f,
2.696683109e-01f,-9.629532695e-01f,-8.715950847e-01f,4.902264774e-01f,9.629532695e-01f,2.696683109e-01f,-4.902264774e-01f,-8.715950847e-01f,
5.000000000e-01f,5.000000000e-01f,5.000000000e-01f,-5.000000000e-01f,5.000000000e-01f,5.000000000e-01f,-1.709618866e-01f,-9.852776527e-01f,
9.065957069e-01f,-4.220002592e-01f,3.426607251e-01f,-9.394592047e-01f,-4.220002592e-01f,-9.065957069e-01f,-9.394592047e-01f,-3.426607251e-01f,
6.438315511e-01f,-7.651672363e-01f,-7.651672363e-01f,-6.438315511e-01f,-6.438315511e-01f,7.651672363e-01f,7.651672363e-01f,6.438315511e-01f,
2.607941031e-01f,-9.653944373e-01f,-8.670462370e-01f,4.982276559e-01f,9.653944373e-01f,2.607941031e-01f,-4.982276559e-01f,-8.670462370e-01f,
5.000000000e-01f,5.000000000e-01f,5.000000000e-01f,-5.000000000e-01f,5.000000000e-01f,5.000000000e-01f,-1.830398887e-01f,-9.831054807e-01f,
9.052967429e-01f,-4.247796834e-01f,3.397768736e-01f,-9.405060410e-01f,-4.247796834e-01f,-9.052967429e-01f,-9.405060410e-01f,-3.397768736e-01f,
6.391244531e-01f,-7.691033483e-01f,-7.691033483e-01f,-6.391244531e-01f,-6.391244531e-01f,7.691033483e-01f,7.691033483e-01f,6.391244531e-01f,
2.518978119e-01f,-9.677538276e-01f,-8.624239564e-01f,5.061866641e-01f,9.677538276e-01f,2.518978119e-01f,-5.061866641e-01f,-8.624239564e-01f,
5.000000000e-01f,5.000000000e-01f,5.000000000e-01f,-5.000000000e-01f,5.000000000e-01f,5.000000000e-01f,-1.950903237e-01f,-9.807852507e-01f,
9.039893150e-01f,-4.275550842e-01f,3.368898630e-01f,-9.415440559e-01f,-4.275550842e-01f,-9.039893150e-01f,-9.415440559e-01f,-3.368898630e-01f,
6.343932748e-01f,-7.730104327e-01f,-7.730104327e-01f,-6.343932748e-01f,-6.343932748e-01f,7.730104327e-01f,7.730104327e-01f,6.343932748e-01f,
2.429801822e-01f,-9.700312614e-01f,-8.577286005e-01f,5.141027570e-01f,9.700312614e-01f,2.429801822e-01f,-5.141027570e-01f,-8.577286005e-01f,
5.000000000e-01f,5.000000000e-01f,5.000000000e-01f,-5.000000000e-01f,5.000000000e-01f,5.000000000e-01f,-2.071113735e-01f,-9.783173800e-01f,
9.026733041e-01f,-4.303264916e-01f,3.339996636e-01f,-9.425731897e-01f,-4.303264916e-01f,-9.026733041e-01f,-9.425731897e-01f,-3.339996636e-01f,
6.296382546e-01f,-7.768884897e-01f,-7.768884897e-01f,-6.296382546e-01f,-6.296382546e-01f,7.768884897e-01f,7.768884897e-01f,6.296382546e-01f,
2.340419590e-01f,-9.722265005e-01f,-8.529605865e-01f,5.219752789e-01f,9.722265005e-01f,2.340419590e-01f,-5.219752789e-01f,-8.529605865e-01f,
5.000000000e-01f,5.000000000e-01f,5.000000000e-01f,-5.000000000e-01f,5.000000000e-01f,5.000000000e-01f,-2.191012353e-01f,-9.757021070e-01f,
9.013488293e-01f,-4.330938160e-01f,3.311063051e-01f,-9.435934424e-01f,-4.330938160e-01f,-9.013488293e-01f,-9.435934424e-01f,-3.311063051e-01f,
6.248595119e-01f,-7.807372212e-01f,-7.807372212e-01f,-6.248595119e-01f,-6.248595119e-01f,7.807372212e-01f,7.807372212e-01f,6.248595119e-01f,
2.250839174e-01f,-9.743393660e-01f,-8.481203318e-01f,5.298036337e-01f,9.743393660e-01f,2.250839174e-01f,-5.298036337e-01f,-8.481203318e-01f,
5.000000000e-01f,5.000000000e-01f,5.000000000e-01f,-5.000000000e-01f,5.000000000e-01f,5.000000000e-01f,-2.310581058e-01f,-9.729399681e-01f,
9.000158906e-01f,-4.358570874e-01f,3.282098472e-01f,-9.446048141e-01f,-4.358570874e-01f,-9.000158906e-01f,-9.446048141e-01f,-3.282098472e-01f,
6.200572252e-01f,-7.845565677e-01f,-7.845565677e-01f,-6.200572252e-01f,-6.200572252e-01f,7.845565677e-01f,7.845565677e-01f,6.200572252e-01f,
2.161068022e-01f,-9.763697386e-01f,-8.432082534e-01f,5.375870466e-01f,9.763697386e-01f,2.161068022e-01f,-5.375870466e-01f,-8.432082534e-01f,
5.000000000e-01f,5.000000000e-01f,5.000000000e-01f,-5.000000000e-01f,5.000000000e-01f,5.000000000e-01f,-2.429801822e-01f,-9.700312614e-01f,
8.986744881e-01f,-4.386162460e-01f,3.253102899e-01f,-9.456073046e-01f,-4.386162460e-01f,-8.986744881e-01f,-9.456073046e-01f,-3.253102899e-01f,
6.152315736e-01f,-7.883464098e-01f,-7.883464098e-01f,-6.152315736e-01f,-6.152315736e-01f,7.883464098e-01f,7.883464098e-01f,6.152315736e-01f,
2.071113735e-01f,-9.783173800e-01f,-8.382247090e-01f,5.453249812e-01f,9.783173800e-01f,2.071113735e-01f,-5.453249812e-01f,-8.382247090e-01f,
5.000000000e-01f,5.000000000e-01f,5.000000000e-01f,-5.000000000e-01f,5.000000000e-01f,5.000000000e-01f,-2.548656464e-01f,-9.669764638e-01f,
8.973245621e-01f,-4.413712621e-01f,3.224076927e-01f,-9.466009140e-01f,-4.413712621e-01f,-8.973245621e-01f,-9.466009140e-01f,-3.224076927e-01f,
6.103827953e-01f,-7.921065688e-01f,-7.921065688e-01f,-6.103827953e-01f,-6.103827953e-01f,7.921065688e-01f,7.921065688e-01f,6.103827953e-01f,
1.980984062e-01f,-9.801821113e-01f,-8.331701756e-01f,5.530167222e-01f,9.801821113e-01f,1.980984062e-01f,-5.530167222e-01f,-8.331701756e-01f,
5.000000000e-01f,5.000000000e-01f,5.000000000e-01f,-5.000000000e-01f,5.000000000e-01f,5.000000000e-01f,-2.667127550e-01f,-9.637760520e-01f,
8.959662318e-01f,-4.441221356e-01f,3.195020258e-01f,-9.475855827e-01f,-4.441221356e-01f,-8.959662318e-01f,-9.475855827e-01f,-3.195020258e-01f,
6.055110693e-01f,-7.958369255e-01f,-7.958369255e-01f,-6.055110693e-01f,-6.055110693e-01f,7.958369255e-01f,7.958369255e-01f,6.055110693e-01f,
1.890686601e-01f,-9.819638729e-01f,-8.280450702e-01f,5.606615543e-01f,9.819638729e-01f,1.890686601e-01f,-5.606615543e-01f,-8.280450702e-01f,
5.000000000e-01f,5.000000000e-01f,5.000000000e-01f,-5.000000000e-01f,5.000000000e-01f,5.000000000e-01f,-2.785196900e-01f,-9.604305029e-01f,
8.945994973e-01f,-4.468688369e-01f,3.165933788e-01f,-9.485613704e-01f,-4.468688369e-01f,-8.945994973e-01f,-9.485613704e-01f,-3.165933788e-01f,
6.006164551e-01f,-7.995372415e-01f,-7.995372415e-01f,-6.006164551e-01f,-6.006164551e-01f,7.995372415e-01f,7.995372415e-01f,6.006164551e-01f,
1.800228953e-01f,-9.836624265e-01f,-8.228498101e-01f,5.682589412e-01f,9.836624265e-01f,1.800228953e-01f,-5.682589412e-01f,-8.228498101e-01f,
5.000000000e-01f,5.000000000e-01f,5.000000000e-01f,-5.000000000e-01f,5.000000000e-01f,5.000000000e-01f,-2.902846634e-01f,-9.569403529e-01f,
8.932242990e-01f,-4.496113360e-01f,3.136817515e-01f,-9.495281577e-01f,-4.496113360e-01f,-8.932242990e-01f,-9.495281577e-01f,-3.136817515e-01f,
5.956993103e-01f,-8.032075167e-01f,-8.032075167e-01f,-5.956993103e-01f,-5.956993103e-01f,8.032075167e-01f,8.032075167e-01f,5.956993103e-01f,
1.709618866e-01f,-9.852776527e-01f,-8.175848126e-01f,5.758081675e-01f,9.852776527e-01f,1.709618866e-01f,-5.758081675e-01f,-8.175848126e-01f,
5.000000000e-01f,5.000000000e-01f,5.000000000e-01f,-5.000000000e-01f,5.000000000e-01f,5.000000000e-01f,-3.020059466e-01f,-9.533060193e-01f,
8.918406963e-01f,-4.523495734e-01f,3.107671440e-01f,-9.504860640e-01f,-4.523495734e-01f,-8.918406963e-01f,-9.504860640e-01f,-3.107671440e-01f,
5.907596946e-01f,-8.068475723e-01f,-8.068475723e-01f,-5.907596946e-01f,-5.907596946e-01f,8.068475723e-01f,8.068475723e-01f,5.907596946e-01f,
1.618863940e-01f,-9.868093729e-01f,-8.122506142e-01f,5.833086371e-01f,9.868093729e-01f,1.618863940e-01f,-5.833086371e-01f,-8.122506142e-01f,
5.000000000e-01f,5.000000000e-01f,5.000000000e-01f,-5.000000000e-01f,5.000000000e-01f,5.000000000e-01f,-3.136817515e-01f,-9.495281577e-01f,
8.904487491e-01f,-4.550835788e-01f,3.078496456e-01f,-9.514350295e-01f,-4.550835788e-01f,-8.904487491e-01f,-9.514350295e-01f,-3.078496456e-01f,
5.857978463e-01f,-8.104571700e-01f,-8.104571700e-01f,-5.857978463e-01f,-5.857978463e-01f,8.104571700e-01f,8.104571700e-01f,5.857978463e-01f,
1.527971923e-01f,-9.882575870e-01f,-8.068475723e-01f,5.907596946e-01f,9.882575870e-01f,1.527971923e-01f,-5.907596946e-01f,-8.068475723e-01f,
5.000000000e-01f,5.000000000e-01f,5.000000000e-01f,-5.000000000e-01f,5.000000000e-01f,5.000000000e-01f,-3.253102899e-01f,-9.456073046e-01f,
8.890483379e-01f,-4.578132927e-01f,3.049292266e-01f,-9.523749948e-01f,-4.578132927e-01f,-8.890483379e-01f,-9.523749948e-01f,-3.049292266e-01f,
5.808139443e-01f,-8.140363097e-01f,-8.140363097e-01f,-5.808139443e-01f,-5.808139443e-01f,8.140363097e-01f,8.140363097e-01f,5.808139443e-01f,
1.436950266e-01f,-9.896219969e-01f,-8.013761640e-01f,5.981606841e-01f,9.896219969e-01f,1.436950266e-01f,-5.981606841e-01f,-8.013761640e-01f,
5.000000000e-01f,5.000000000e-01f,5.000000000e-01f,-5.000000000e-01f,5.000000000e-01f,5.000000000e-01f,-3.368898630e-01f,-9.415440559e-01f,
8.876396418e-01f,-4.605387151e-01f,3.020059466e-01f,-9.533060193e-01f,-4.605387151e-01f,-8.876396418e-01f,-9.533060193e-01f,-3.020059466e-01f,
5.758081675e-01f,-8.175848126e-01f,-8.175848126e-01f,-5.758081675e-01f,-5.758081675e-01f,8.175848126e-01f,8.175848126e-01f,5.758081675e-01f,
1.345807016e-01f,-9.909026623e-01f,-7.958369255e-01f,6.055110693e-01f,9.909026623e-01f,1.345807016e-01f,-6.055110693e-01f,-7.958369255e-01f,
5.000000000e-01f,5.000000000e-01f,5.000000000e-01f,-5.000000000e-01f,5.000000000e-01f,5.000000000e-01f,-3.484186828e-01f,-9.373390079e-01f,
8.862225413e-01f,-4.632597864e-01f,2.990798354e-01f,-9.542281032e-01f,-4.632597864e-01f,-8.862225413e-01f,-9.542281032e-01f,-2.990798354e-01f,
5.707807541e-01f,-8.211025000e-01f,-8.211025000e-01f,-5.707807541e-01f,-5.707807541e-01f,8.211025000e-01f,8.211025000e-01f,5.707807541e-01f,
1.254549772e-01f,-9.920992851e-01f,-7.902302146e-01f,6.128100753e-01f,9.920992851e-01f,1.254549772e-01f,-6.128100753e-01f,-7.902302146e-01f,
5.000000000e-01f,5.000000000e-01f,5.000000000e-01f,-5.000000000e-01f,5.000000000e-01f,5.000000000e-01f,-3.598950505e-01f,-9.329928160e-01f,
8.847970963e-01f,-4.659765065e-01f,2.961508930e-01f,-9.551411867e-01f,-4.659765065e-01f,-8.847970963e-01f,-9.551411867e-01f,-2.961508930e-01f,
5.657318234e-01f,-8.245893121e-01f,-8.245893121e-01f,-5.657318234e-01f,-5.657318234e-01f,8.245893121e-01f,8.245893121e-01f,5.657318234e-01f,
1.163186282e-01f,-9.932119250e-01f,-7.845565677e-01f,6.200572252e-01f,9.932119250e-01f,1.163186282e-01f,-6.200572252e-01f,-7.845565677e-01f,
5.000000000e-01f,5.000000000e-01f,5.000000000e-01f,-5.000000000e-01f,5.000000000e-01f,5.000000000e-01f,-3.713172078e-01f,-9.285060763e-01f,
8.833633661e-01f,-4.686888158e-01f,2.932191491e-01f,-9.560452700e-01f,-4.686888158e-01f,-8.833633661e-01f,-9.560452700e-01f,-2.932191491e-01f,
5.606615543e-01f,-8.280450702e-01f,-8.280450702e-01f,-5.606615543e-01f,-5.606615543e-01f,8.280450702e-01f,8.280450702e-01f,5.606615543e-01f,
1.071724221e-01f,-9.942404628e-01f,-7.788165212e-01f,6.272518039e-01f,9.942404628e-01f,1.071724221e-01f,-6.272518039e-01f,-7.788165212e-01f,
5.000000000e-01f,5.000000000e-01f,5.000000000e-01f,-5.000000000e-01f,5.000000000e-01f,5.000000000e-01f,-3.826834261e-01f,-9.238795042e-01f,
8.819212914e-01f,-4.713967443e-01f,2.902846634e-01f,-9.569403529e-01f,-4.713967443e-01f,-8.819212914e-01f,-9.569403529e-01f,-2.902846634e-01f,
5.555702448e-01f,-8.314695954e-01f,-8.314695954e-01f,-5.555702448e-01f,-5.555702448e-01f,8.314695954e-01f,8.314695954e-01f,5.555702448e-01f,
9.801714122e-02f,-9.951847196e-01f,-7.730104327e-01f,6.343932748e-01f,9.951847196e-01f,9.801714122e-02f,-6.343932748e-01f,-7.730104327e-01f,
5.000000000e-01f,5.000000000e-01f,5.000000000e-01f,-5.000000000e-01f,5.000000000e-01f,5.000000000e-01f,-3.939920366e-01f,-9.191138744e-01f,
8.804708719e-01f,-4.741002023e-01f,2.873474658e-01f,-9.578264356e-01f,-4.741002023e-01f,-8.804708719e-01f,-9.578264356e-01f,-2.873474658e-01f,
5.504579544e-01f,-8.348628879e-01f,-8.348628879e-01f,-5.504579544e-01f,-5.504579544e-01f,8.348628879e-01f,8.348628879e-01f,5.504579544e-01f,
8.885355294e-02f,-9.960446954e-01f,-7.671388984e-01f,6.414810419e-01f,9.960446954e-01f,8.885355294e-02f,-6.414810419e-01f,-7.671388984e-01f,
5.000000000e-01f,5.000000000e-01f,5.000000000e-01f,-5.000000000e-01f,5.000000000e-01f,5.000000000e-01f,-4.052413106e-01f,-9.142097831e-01f,
8.790122271e-01f,-4.767992198e-01f,2.844075263e-01f,-9.587034583e-01f,-4.767992198e-01f,-8.790122271e-01f,-9.587034583e-01f,-2.844075263e-01f,
5.453249812e-01f,-8.382247090e-01f,-8.382247090e-01f,-5.453249812e-01f,-5.453249812e-01f,8.382247090e-01f,8.382247090e-01f,5.453249812e-01f,
7.968243957e-02f,-9.968202710e-01f,-7.612023950e-01f,6.485143900e-01f,9.968202710e-01f,7.968243957e-02f,-6.485143900e-01f,-7.612023950e-01f,
5.000000000e-01f,5.000000000e-01f,5.000000000e-01f,-5.000000000e-01f,5.000000000e-01f,5.000000000e-01f,-4.164295495e-01f,-9.091680050e-01f,
8.775452971e-01f,-4.794937670e-01f,2.814649343e-01f,-9.595715404e-01f,-4.794937670e-01f,-8.775452971e-01f,-9.595715404e-01f,-2.814649343e-01f,
5.401714444e-01f,-8.415549994e-01f,-8.415549994e-01f,-5.401714444e-01f,-5.401714444e-01f,8.415549994e-01f,8.415549994e-01f,5.401714444e-01f,
7.050457597e-02f,-9.975114465e-01f,-7.552013993e-01f,6.554928422e-01f,9.975114465e-01f,7.050457597e-02f,-6.554928422e-01f,-7.552013993e-01f,
5.000000000e-01f,5.000000000e-01f,5.000000000e-01f,-5.000000000e-01f,5.000000000e-01f,5.000000000e-01f,-4.275550842e-01f,-9.039893150e-01f,
8.760700822e-01f,-4.821837842e-01f,2.785196900e-01f,-9.604305029e-01f,-4.821837842e-01f,-8.760700822e-01f,-9.604305029e-01f,-2.785196900e-01f,
5.349976420e-01f,-8.448535800e-01f,-8.448535800e-01f,-5.349976420e-01f,-5.349976420e-01f,8.448535800e-01f,8.448535800e-01f,5.349976420e-01f,
6.132073700e-02f,-9.981181026e-01f,-7.491363883e-01f,6.624158025e-01f,9.981181026e-01f,6.132073700e-02f,-6.624158025e-01f,-7.491363883e-01f,
5.000000000e-01f,5.000000000e-01f,5.000000000e-01f,-5.000000000e-01f,5.000000000e-01f,5.000000000e-01f,-4.386162460e-01f,-8.986744881e-01f,
8.745866418e-01f,-4.848692417e-01f,2.755718231e-01f,-9.612804651e-01f,-4.848692417e-01f,-8.745866418e-01f,-9.612804651e-01f,-2.755718231e-01f,
5.298036337e-01f,-8.481203318e-01f,-8.481203318e-01f,-5.298036337e-01f,-5.298036337e-01f,8.481203318e-01f,8.481203318e-01f,5.298036337e-01f,
5.213170499e-02f,-9.986402392e-01f,-7.430079579e-01f,6.692826152e-01f,9.986402392e-01f,5.213170499e-02f,-6.692826152e-01f,-7.430079579e-01f,
5.000000000e-01f,5.000000000e-01f,5.000000000e-01f,-5.000000000e-01f,5.000000000e-01f,5.000000000e-01f,-4.496113360e-01f,-8.932242990e-01f,
8.730949759e-01f,-4.875501692e-01f,2.726213634e-01f,-9.621214271e-01f,-4.875501692e-01f,-8.730949759e-01f,-9.621214271e-01f,-2.726213634e-01f,
5.245896578e-01f,-8.513551950e-01f,-8.513551950e-01f,-5.245896578e-01f,-5.245896578e-01f,8.513551950e-01f,8.513551950e-01f,5.245896578e-01f,
4.293825850e-02f,-9.990777373e-01f,-7.368165851e-01f,6.760926843e-01f,9.990777373e-01f,4.293825850e-02f,-6.760926843e-01f,-7.368165851e-01f,
5.000000000e-01f,5.000000000e-01f,5.000000000e-01f,-5.000000000e-01f,5.000000000e-01f,5.000000000e-01f,-4.605387151e-01f,-8.876396418e-01f,
8.715950847e-01f,-4.902264774e-01f,2.696683109e-01f,-9.629532695e-01f,-4.902264774e-01f,-8.715950847e-01f,-9.629532695e-01f,-2.696683109e-01f,
5.193560123e-01f,-8.545579910e-01f,-8.545579910e-01f,-5.193560123e-01f,-5.193560123e-01f,8.545579910e-01f,8.545579910e-01f,5.193560123e-01f,
3.374117240e-02f,-9.994305968e-01f,-7.305627465e-01f,6.828455329e-01f,9.994305968e-01f,3.374117240e-02f,-6.828455329e-01f,-7.305627465e-01f,
5.000000000e-01f,5.000000000e-01f,5.000000000e-01f,-5.000000000e-01f,5.000000000e-01f,5.000000000e-01f,-4.713967443e-01f,-8.819212914e-01f,
8.700869679e-01f,-4.928981960e-01f,2.667127550e-01f,-9.637760520e-01f,-4.928981960e-01f,-8.700869679e-01f,-9.637760520e-01f,-2.667127550e-01f,
5.141027570e-01f,-8.577286005e-01f,-8.577286005e-01f,-5.141027570e-01f,-5.141027570e-01f,8.577286005e-01f,8.577286005e-01f,5.141027570e-01f,
2.454122901e-02f,-9.996988177e-01f,-7.242470980e-01f,6.895405650e-01f,9.996988177e-01f,2.454122901e-02f,-6.895405650e-01f,-7.242470980e-01f,
5.000000000e-01f,5.000000000e-01f,5.000000000e-01f,-5.000000000e-01f,5.000000000e-01f,5.000000000e-01f,-4.821837842e-01f,-8.760700822e-01f,
8.685706854e-01f,-4.955652654e-01f,2.637546659e-01f,-9.645897746e-01f,-4.955652654e-01f,-8.685706854e-01f,-9.645897746e-01f,-2.637546659e-01f,
5.088301301e-01f,-8.608669639e-01f,-8.608669639e-01f,-5.088301301e-01f,-5.088301301e-01f,8.608669639e-01f,8.608669639e-01f,5.088301301e-01f,
1.533920597e-02f,-9.998823404e-01f,-7.178700566e-01f,6.961771250e-01f,9.998823404e-01f,1.533920597e-02f,-6.961771250e-01f,-7.178700566e-01f,
5.000000000e-01f,5.000000000e-01f,5.000000000e-01f,-5.000000000e-01f,5.000000000e-01f,5.000000000e-01f,-4.928981960e-01f,-8.700869679e-01f,
8.670462370e-01f,-4.982276559e-01f,2.607941031e-01f,-9.653944373e-01f,-4.982276559e-01f,-8.670462370e-01f,-9.653944373e-01f,-2.607941031e-01f,
5.035383701e-01f,-8.639728427e-01f,-8.639728427e-01f,-5.035383701e-01f,-5.035383701e-01f,8.639728427e-01f,8.639728427e-01f,5.035383701e-01f,
6.135884672e-03f,-9.999811649e-01f,-7.114322186e-01f,7.027547359e-01f,9.999811649e-01f,6.135884672e-03f,-7.027547359e-01f,-7.114322186e-01f,
5.000000000e-01f,5.000000000e-01f,5.000000000e-01f,-5.000000000e-01f,5.000000000e-01f,5.000000000e-01f,-5.035383701e-01f,-8.639728427e-01f,
8.655136228e-01f,-5.008853674e-01f,2.578310966e-01f,-9.661899805e-01f,-5.008853674e-01f,-8.655136228e-01f,-9.661899805e-01f,-2.578310966e-01f,
4.982276559e-01f,-8.670462370e-01f,-8.670462370e-01f,-4.982276559e-01f,-4.982276559e-01f,8.670462370e-01f,8.670462370e-01f,4.982276559e-01f,
-3.067956772e-03f,-9.999952912e-01f,-7.049340606e-01f,7.092728019e-01f,9.999952912e-01f,-3.067956772e-03f,-7.092728019e-01f,-7.049340606e-01f,
5.000000000e-01f,5.000000000e-01f,5.000000000e-01f,-5.000000000e-01f,5.000000000e-01f,5.000000000e-01f,-5.141027570e-01f,-8.577286005e-01f,
8.639728427e-01f,-5.035383701e-01f,2.548656464e-01f,-9.669764638e-01f,-5.035383701e-01f,-8.639728427e-01f,-9.669764638e-01f,-2.548656464e-01f,
4.928981960e-01f,-8.700869679e-01f,-8.700869679e-01f,-4.928981960e-01f,-4.928981960e-01f,8.700869679e-01f,8.700869679e-01f,4.928981960e-01f,
-1.227153838e-02f,-9.999247193e-01f,-6.983762383e-01f,7.157308459e-01f,9.999247193e-01f,-1.227153838e-02f,-7.157308459e-01f,-6.983762383e-01f,
5.000000000e-01f,5.000000000e-01f,5.000000000e-01f,-5.000000000e-01f,5.000000000e-01f,5.000000000e-01f,-5.245896578e-01f,-8.513551950e-01f,
8.624239564e-01f,-5.061866641e-01f,2.518978119e-01f,-9.677538276e-01f,-5.061866641e-01f,-8.624239564e-01f,-9.677538276e-01f,-2.518978119e-01f,
4.875501692e-01f,-8.730949759e-01f,-8.730949759e-01f,-4.875501692e-01f,-4.875501692e-01f,8.730949759e-01f,8.730949759e-01f,4.875501692e-01f,
-2.147408016e-02f,-9.997693896e-01f,-6.917592287e-01f,7.221282125e-01f,9.997693896e-01f,-2.147408016e-02f,-7.221282125e-01f,-6.917592287e-01f,
5.000000000e-01f,5.000000000e-01f,5.000000000e-01f,-5.000000000e-01f,5.000000000e-01f,5.000000000e-01f,-5.349976420e-01f,-8.448535800e-01f,
8.608669639e-01f,-5.088301301e-01f,2.489276081e-01f,-9.685220718e-01f,-5.088301301e-01f,-8.608669639e-01f,-9.685220718e-01f,-2.489276081e-01f,
4.821837842e-01f,-8.760700822e-01f,-8.760700822e-01f,-4.821837842e-01f,-4.821837842e-01f,8.760700822e-01f,8.760700822e-01f,4.821837842e-01f,
-3.067480400e-02f,-9.995294213e-01f,-6.850836873e-01f,7.284643650e-01f,9.995294213e-01f,-3.067480400e-02f,-7.284643650e-01f,-6.850836873e-01f,
5.000000000e-01f,5.000000000e-01f,5.000000000e-01f,-5.000000000e-01f,5.000000000e-01f,5.000000000e-01f,-5.453249812e-01f,-8.382247090e-01f,
8.593018055e-01f,-5.114688277e-01f,2.459550500e-01f,-9.692812562e-01f,-5.114688277e-01f,-8.593018055e-01f,-9.692812562e-01f,-2.459550500e-01f,
4.767992198e-01f,-8.790122271e-01f,-8.790122271e-01f,-4.767992198e-01f,-4.767992198e-01f,8.790122271e-01f,8.790122271e-01f,4.767992198e-01f,
-3.987292573e-02f,-9.992047548e-01f,-6.783500314e-01f,7.347388864e-01f,9.992047548e-01f,-3.987292573e-02f,-7.347388864e-01f,-6.783500314e-01f,
5.000000000e-01f,5.000000000e-01f,5.000000000e-01f,-5.000000000e-01f,5.000000000e-01f,5.000000000e-01f,-5.555702448e-01f,-8.314695954e-01f,
8.577286005e-01f,-5.141027570e-01f,2.429801822e-01f,-9.700312614e-01f,-5.141027570e-01f,-8.577286005e-01f,-9.700312614e-01f,-2.429801822e-01f,
4.713967443e-01f,-8.819212914e-01f,-8.819212914e-01f,-4.713967443e-01f,-4.713967443e-01f,8.819212914e-01f,8.819212914e-01f,4.713967443e-01f,
-4.906767607e-02f,-9.987954497e-01f,-6.715589762e-01f,7.409511209e-01f,9.987954497e-01f,-4.906767607e-02f,-7.409511209e-01f,-6.715589762e-01f,
5.000000000e-01f,5.000000000e-01f,5.000000000e-01f,-5.000000000e-01f,5.000000000e-01f,5.000000000e-01f,-5.657318234e-01f,-8.245893121e-01f,
8.561473489e-01f,-5.167317986e-01f,2.400030196e-01f,-9.707721472e-01f,-5.167317986e-01f,-8.561473489e-01f,-9.707721472e-01f,-2.400030196e-01f,
4.659765065e-01f,-8.847970963e-01f,-8.847970963e-01f,-4.659765065e-01f,-4.659765065e-01f,8.847970963e-01f,8.847970963e-01f,4.659765065e-01f,
-5.825826526e-02f,-9.983015656e-01f,-6.647109985e-01f,7.471005917e-01f,9.983015656e-01f,-5.825826526e-02f,-7.471005917e-01f,-6.647109985e-01f,
5.000000000e-01f,5.000000000e-01f,5.000000000e-01f,-5.000000000e-01f,5.000000000e-01f,5.000000000e-01f,-5.758081675e-01f,-8.175848126e-01f,
8.545579910e-01f,-5.193560123e-01f,2.370236069e-01f,-9.715039134e-01f,-5.193560123e-01f,-8.545579910e-01f,-9.715039134e-01f,-2.370236069e-01f,
4.605387151e-01f,-8.876396418e-01f,-8.876396418e-01f,-4.605387151e-01f,-4.605387151e-01f,8.876396418e-01f,8.876396418e-01f,4.605387151e-01f,
-6.744392216e-02f,-9.977230430e-01f,-6.578066945e-01f,7.531868219e-01f,9.977230430e-01f,-6.744392216e-02f,-7.531868219e-01f,-6.578066945e-01f,
5.000000000e-01f,5.000000000e-01f,5.000000000e-01f,-5.000000000e-01f,5.000000000e-01f,5.000000000e-01f,-5.857978463e-01f,-8.104571700e-01f,
8.529605865e-01f,-5.219752789e-01f,2.340419590e-01f,-9.722265005e-01f,-5.219752789e-01f,-8.529605865e-01f,-9.722265005e-01f,-2.340419590e-01f,
4.550835788e-01f,-8.904487491e-01f,-8.904487491e-01f,-4.550835788e-01f,-4.550835788e-01f,8.904487491e-01f,8.904487491e-01f,4.550835788e-01f,
-7.662386447e-02f,-9.970600605e-01f,-6.508466601e-01f,7.592092156e-01f,9.970600605e-01f,-7.662386447e-02f,-7.592092156e-01f,-6.508466601e-01f,
5.000000000e-01f,5.000000000e-01f,5.000000000e-01f,-5.000000000e-01f,5.000000000e-01f,5.000000000e-01f,-5.956993103e-01f,-8.032075167e-01f,
8.513551950e-01f,-5.245896578e-01f,2.310581058e-01f,-9.729399681e-01f,-5.245896578e-01f,-8.513551950e-01f,-9.729399681e-01f,-2.310581058e-01f,
4.496113360e-01f,-8.932242990e-01f,-8.932242990e-01f,-4.496113360e-01f,-4.496113360e-01f,8.932242990e-01f,8.932242990e-01f,4.496113360e-01f,
-8.579730988e-02f,-9.963126183e-01f,-6.438315511e-01f,7.651672363e-01f,9.963126183e-01f,-8.579730988e-02f,-7.651672363e-01f,-6.438315511e-01f,
5.000000000e-01f,5.000000000e-01f,5.000000000e-01f,-5.000000000e-01f,5.000000000e-01f,5.000000000e-01f,-6.055110693e-01f,-7.958369255e-01f,
8.497417569e-01f,-5.271991491e-01f,2.280720770e-01f,-9.736442566e-01f,-5.271991491e-01f,-8.497417569e-01f,-9.736442566e-01f,-2.280720770e-01f,
4.441221356e-01f,-8.959662318e-01f,-8.959662318e-01f,-4.441221356e-01f,-4.441221356e-01f,8.959662318e-01f,8.959662318e-01f,4.441221356e-01f,
-9.496349841e-02f,-9.954807758e-01f,-6.367618442e-01f,7.710605264e-01f,9.954807758e-01f,-9.496349841e-02f,-7.710605264e-01f,-6.367618442e-01f,
5.000000000e-01f,5.000000000e-01f,5.000000000e-01f,-5.000000000e-01f,5.000000000e-01f,5.000000000e-01f,-6.152315736e-01f,-7.883464098e-01f,
8.481203318e-01f,-5.298036337e-01f,2.250839174e-01f,-9.743393660e-01f,-5.298036337e-01f,-8.481203318e-01f,-9.743393660e-01f,-2.250839174e-01f,
4.386162460e-01f,-8.986744881e-01f,-8.986744881e-01f,-4.386162460e-01f,-4.386162460e-01f,8.986744881e-01f,8.986744881e-01f,4.386162460e-01f,
-1.041216329e-01f,-9.945645928e-01f,-6.296382546e-01f,7.768884897e-01f,9.945645928e-01f,-1.041216329e-01f,-7.768884897e-01f,-6.296382546e-01f,
5.000000000e-01f,5.000000000e-01f,5.000000000e-01f,-5.000000000e-01f,5.000000000e-01f,5.000000000e-01f,-6.248595119e-01f,-7.807372212e-01f,
8.464909196e-01f,-5.324031115e-01f,2.220936269e-01f,-9.750253558e-01f,-5.324031115e-01f,-8.464909196e-01f,-9.750253558e-01f,-2.220936269e-01f,
4.330938160e-01f,-9.013488293e-01f,-9.013488293e-01f,-4.330938160e-01f,-4.330938160e-01f,9.013488293e-01f,9.013488293e-01f,4.330938160e-01f,
-1.132709533e-01f,-9.935641289e-01f,-6.224612594e-01f,7.826505899e-01f,9.935641289e-01f,-1.132709533e-01f,-7.826505899e-01f,-6.224612594e-01f,
5.000000000e-01f,5.000000000e-01f,5.000000000e-01f,-5.000000000e-01f,5.000000000e-01f,5.000000000e-01f,-6.343932748e-01f,-7.730104327e-01f,
8.448535800e-01f,-5.349976420e-01f,2.191012353e-01f,-9.757021070e-01f,-5.349976420e-01f,-8.448535800e-01f,-9.757021070e-01f,-2.191012353e-01f,
4.275550842e-01f,-9.039893150e-01f,-9.039893150e-01f,-4.275550842e-01f,-4.275550842e-01f,9.039893150e-01f,9.039893150e-01f,4.275550842e-01f,
-1.224106774e-01f,-9.924795628e-01f,-6.152315736e-01f,7.883464098e-01f,9.924795628e-01f,-1.224106774e-01f,-7.883464098e-01f,-6.152315736e-01f,
5.000000000e-01f,5.000000000e-01f,5.000000000e-01f,-5.000000000e-01f,5.000000000e-01f,5.000000000e-01f,-6.438315511e-01f,-7.651672363e-01f,
8.432082534e-01f,-5.375870466e-01f,2.161068022e-01f,-9.763697386e-01f,-5.375870466e-01f,-8.432082534e-01f,-9.763697386e-01f,-2.161068022e-01f,
4.220002592e-01f,-9.065957069e-01f,-9.065957069e-01f,-4.220002592e-01f,-4.220002592e-01f,9.065957069e-01f,9.065957069e-01f,4.220002592e-01f,
-1.315400302e-01f,-9.913108349e-01f,-6.079497933e-01f,7.939754725e-01f,9.913108349e-01f,-1.315400302e-01f,-7.939754725e-01f,-6.079497933e-01f,
5.000000000e-01f,5.000000000e-01f,5.000000000e-01f,-5.000000000e-01f,5.000000000e-01f,5.000000000e-01f,-6.531728506e-01f,-7.572088242e-01f,
8.415549994e-01f,-5.401714444e-01f,2.131103128e-01f,-9.770281315e-01f,-5.401714444e-01f,-8.415549994e-01f,-9.770281315e-01f,-2.131103128e-01f,
4.164295495e-01f,-9.091680050e-01f,-9.091680050e-01f,-4.164295495e-01f,-4.164295495e-01f,9.091680050e-01f,9.091680050e-01f,4.164295495e-01f,
-1.406582445e-01f,-9.900581837e-01f,-6.006164551e-01f,7.995372415e-01f,9.900581837e-01f,-1.406582445e-01f,-7.995372415e-01f,-6.006164551e-01f,
5.000000000e-01f,5.000000000e-01f,5.000000000e-01f,-5.000000000e-01f,5.000000000e-01f,5.000000000e-01f,-6.624158025e-01f,-7.491363883e-01f,
8.398938179e-01f,-5.427507758e-01f,2.101118416e-01f,-9.776773453e-01f,-5.427507758e-01f,-8.398938179e-01f,-9.776773453e-01f,-2.101118416e-01f,
4.108431637e-01f,-9.117060304e-01f,-9.117060304e-01f,-4.108431637e-01f,-4.108431637e-01f,9.117060304e-01f,9.117060304e-01f,4.108431637e-01f,
-1.497645378e-01f,-9.887216687e-01f,-5.932322741e-01f,8.050313592e-01f,9.887216687e-01f,-1.497645378e-01f,-8.050313592e-01f,-5.932322741e-01f,
5.000000000e-01f,5.000000000e-01f,5.000000000e-01f,-5.000000000e-01f,5.000000000e-01f,5.000000000e-01f,-6.715589762e-01f,-7.409511209e-01f,
8.382247090e-01f,-5.453249812e-01f,2.071113735e-01f,-9.783173800e-01f,-5.453249812e-01f,-8.382247090e-01f,-9.783173800e-01f,-2.071113735e-01f,
4.052413106e-01f,-9.142097831e-01f,-9.142097831e-01f,-4.052413106e-01f,-4.052413106e-01f,9.142097831e-01f,9.142097831e-01f,4.052413106e-01f,
-1.588581502e-01f,-9.873014092e-01f,-5.857978463e-01f,8.104571700e-01f,9.873014092e-01f,-1.588581502e-01f,-8.104571700e-01f,-5.857978463e-01f,
5.000000000e-01f,5.000000000e-01f,5.000000000e-01f,-5.000000000e-01f,5.000000000e-01f,5.000000000e-01f,-6.806010008e-01f,-7.326542735e-01f,
8.365477324e-01f,-5.478940606e-01f,2.041089684e-01f,-9.789481759e-01f,-5.478940606e-01f,-8.365477324e-01f,-9.789481759e-01f,-2.041089684e-01f,
3.996241987e-01f,-9.166790843e-01f,-9.166790843e-01f,-3.996241987e-01f,-3.996241987e-01f,9.166790843e-01f,9.166790843e-01f,3.996241987e-01f,
-1.679382920e-01f,-9.857975245e-01f,-5.783137679e-01f,8.158144355e-01f,9.857975245e-01f,-1.679382920e-01f,-8.158144355e-01f,-5.783137679e-01f,
5.000000000e-01f,5.000000000e-01f,5.000000000e-01f,-5.000000000e-01f,5.000000000e-01f,5.000000000e-01f,-6.895405650e-01f,-7.242470980e-01f,
8.348628879e-01f,-5.504579544e-01f,2.011046410e-01f,-9.795697927e-01f,-5.504579544e-01f,-8.348628879e-01f,-9.795697927e-01f,-2.011046410e-01f,
3.939920366e-01f,-9.191138744e-01f,-9.191138744e-01f,-3.939920366e-01f,-3.939920366e-01f,9.191138744e-01f,9.191138744e-01f,3.939920366e-01f,
-1.770042181e-01f,-9.842100739e-01f,-5.707807541e-01f,8.211025000e-01f,9.842100739e-01f,-1.770042181e-01f,-8.211025000e-01f,-5.707807541e-01f,
5.000000000e-01f,5.000000000e-01f,5.000000000e-01f,-5.000000000e-01f,5.000000000e-01f,5.000000000e-01f,-6.983762383e-01f,-7.157308459e-01f,
8.331701756e-01f,-5.530167222e-01f,1.980984062e-01f,-9.801821113e-01f,-5.530167222e-01f,-8.331701756e-01f,-9.801821113e-01f,-1.980984062e-01f,
3.883450329e-01f,-9.215140343e-01f,-9.215140343e-01f,-3.883450329e-01f,-3.883450329e-01f,9.215140343e-01f,9.215140343e-01f,3.883450329e-01f,
-1.860551536e-01f,-9.825392962e-01f,-5.631993413e-01f,8.263210654e-01f,9.825392962e-01f,-1.860551536e-01f,-8.263210654e-01f,-5.631993413e-01f,
5.000000000e-01f,5.000000000e-01f,5.000000000e-01f,-5.000000000e-01f,5.000000000e-01f,5.000000000e-01f,-7.071067691e-01f,-7.071067691e-01f,
8.314695954e-01f,-5.555702448e-01f,1.950903237e-01f,-9.807852507e-01f,-5.555702448e-01f,-8.314695954e-01f,-9.807852507e-01f,-1.950903237e-01f,
3.826834261e-01f,-9.238795042e-01f,-9.238795042e-01f,-3.826834261e-01f,-3.826834261e-01f,9.238795042e-01f,9.238795042e-01f,3.826834261e-01f,
-1.950903237e-01f,-9.807852507e-01f,-5.555702448e-01f,8.314695954e-01f,9.807852507e-01f,-1.950903237e-01f,-8.314695954e-01f,-5.555702448e-01f,
5.000000000e-01f,5.000000000e-01f,5.000000000e-01f,-5.000000000e-01f,5.000000000e-01f,5.000000000e-01f,-7.157308459e-01f,-6.983762383e-01f,
8.297612071e-01f,-5.581185222e-01f,1.920803934e-01f,-9.813792109e-01f,-5.581185222e-01f,-8.297612071e-01f,-9.813792109e-01f,-1.920803934e-01f,
3.770074248e-01f,-9.262102246e-01f,-9.262102246e-01f,-3.770074248e-01f,-3.770074248e-01f,9.262102246e-01f,9.262102246e-01f,3.770074248e-01f,
-2.041089684e-01f,-9.789481759e-01f,-5.478940606e-01f,8.365477324e-01f,9.789481759e-01f,-2.041089684e-01f,-8.365477324e-01f,-5.478940606e-01f,
5.000000000e-01f,5.000000000e-01f,5.000000000e-01f,-5.000000000e-01f,5.000000000e-01f,5.000000000e-01f,-7.242470980e-01f,-6.895405650e-01f,
8.280450702e-01f,-5.606615543e-01f,1.890686601e-01f,-9.819638729e-01f,-5.606615543e-01f,-8.280450702e-01f,-9.819638729e-01f,-1.890686601e-01f,
3.713172078e-01f,-9.285060763e-01f,-9.285060763e-01f,-3.713172078e-01f,-3.713172078e-01f,9.285060763e-01f,9.285060763e-01f,3.713172078e-01f,
-2.131103128e-01f,-9.770281315e-01f,-5.401714444e-01f,8.415549994e-01f,9.770281315e-01f,-2.131103128e-01f,-8.415549994e-01f,-5.401714444e-01f,
5.000000000e-01f,5.000000000e-01f,5.000000000e-01f,-5.000000000e-01f,5.000000000e-01f,5.000000000e-01f,-7.326542735e-01f,-6.806010008e-01f,
8.263210654e-01f,-5.631993413e-01f,1.860551536e-01f,-9.825392962e-01f,-5.631993413e-01f,-8.263210654e-01f,-9.825392962e-01f,-1.860551536e-01f,
3.656129837e-01f,-9.307669401e-01f,-9.307669401e-01f,-3.656129837e-01f,-3.656129837e-01f,9.307669401e-01f,9.307669401e-01f,3.656129837e-01f,
-2.220936269e-01f,-9.750253558e-01f,-5.324031115e-01f,8.464909196e-01f,9.750253558e-01f,-2.220936269e-01f,-8.464909196e-01f,-5.324031115e-01f,
5.000000000e-01f,5.000000000e-01f,5.000000000e-01f,-5.000000000e-01f,5.000000000e-01f,5.000000000e-01f,-7.409511209e-01f,-6.715589762e-01f,
8.245893121e-01f,-5.657318234e-01f,1.830398887e-01f,-9.831054807e-01f,-5.657318234e-01f,-8.245893121e-01f,-9.831054807e-01f,-1.830398887e-01f,
3.598950505e-01f,-9.329928160e-01f,-9.329928160e-01f,-3.598950505e-01f,-3.598950505e-01f,9.329928160e-01f,9.329928160e-01f,3.598950505e-01f,
-2.310581058e-01f,-9.729399681e-01f,-5.245896578e-01f,8.513551950e-01f,9.729399681e-01f,-2.310581058e-01f,-8.513551950e-01f,-5.245896578e-01f,
5.000000000e-01f,5.000000000e-01f,5.000000000e-01f,-5.000000000e-01f,5.000000000e-01f,5.000000000e-01f,-7.491363883e-01f,-6.624158025e-01f,
8.228498101e-01f,-5.682589412e-01f,1.800228953e-01f,-9.836624265e-01f,-5.682589412e-01f,-8.228498101e-01f,-9.836624265e-01f,-1.800228953e-01f,
3.541635275e-01f,-9.351835251e-01f,-9.351835251e-01f,-3.541635275e-01f,-3.541635275e-01f,9.351835251e-01f,9.351835251e-01f,3.541635275e-01f,
-2.400030196e-01f,-9.707721472e-01f,-5.167317986e-01f,8.561473489e-01f,9.707721472e-01f,-2.400030196e-01f,-8.561473489e-01f,-5.167317986e-01f,
5.000000000e-01f,5.000000000e-01f,5.000000000e-01f,-5.000000000e-01f,5.000000000e-01f,5.000000000e-01f,-7.572088242e-01f,-6.531728506e-01f,
8.211025000e-01f,-5.707807541e-01f,1.770042181e-01f,-9.842100739e-01f,-5.707807541e-01f,-8.211025000e-01f,-9.842100739e-01f,-1.770042181e-01f,
3.484186828e-01f,-9.373390079e-01f,-9.373390079e-01f,-3.484186828e-01f,-3.484186828e-01f,9.373390079e-01f,9.373390079e-01f,3.484186828e-01f,
-2.489276081e-01f,-9.685220718e-01f,-5.088301301e-01f,8.608669639e-01f,9.685220718e-01f,-2.489276081e-01f,-8.608669639e-01f,-5.088301301e-01f,
5.000000000e-01f,5.000000000e-01f,5.000000000e-01f,-5.000000000e-01f,5.000000000e-01f,5.000000000e-01f,-7.651672363e-01f,-6.438315511e-01f,
8.193475008e-01f,-5.732971430e-01f,1.739838719e-01f,-9.847484827e-01f,-5.732971430e-01f,-8.193475008e-01f,-9.847484827e-01f,-1.739838719e-01f,
3.426607251e-01f,-9.394592047e-01f,-9.394592047e-01f,-3.426607251e-01f,-3.426607251e-01f,9.394592047e-01f,9.394592047e-01f,3.426607251e-01f,
-2.578310966e-01f,-9.661899805e-01f,-5.008853674e-01f,8.655136228e-01f,9.661899805e-01f,-2.578310966e-01f,-8.655136228e-01f,-5.008853674e-01f,
5.000000000e-01f,5.000000000e-01f,5.000000000e-01f,-5.000000000e-01f,5.000000000e-01f,5.000000000e-01f,-7.730104327e-01f,-6.343932748e-01f,
8.175848126e-01f,-5.758081675e-01f,1.709618866e-01f,-9.852776527e-01f,-5.758081675e-01f,-8.175848126e-01f,-9.852776527e-01f,-1.709618866e-01f,
3.368898630e-01f,-9.415440559e-01f,-9.415440559e-01f,-3.368898630e-01f,-3.368898630e-01f,9.415440559e-01f,9.415440559e-01f,3.368898630e-01f,
-2.667127550e-01f,-9.637760520e-01f,-4.928981960e-01f,8.700869679e-01f,9.637760520e-01f,-2.667127550e-01f,-8.700869679e-01f,-4.928981960e-01f,
5.000000000e-01f,5.000000000e-01f,5.000000000e-01f,-5.000000000e-01f,5.000000000e-01f,5.000000000e-01f,-7.807372212e-01f,-6.248595119e-01f,
8.158144355e-01f,-5.783137679e-01f,1.679382920e-01f,-9.857975245e-01f,-5.783137679e-01f,-8.158144355e-01f,-9.857975245e-01f,-1.679382920e-01f,
3.311063051e-01f,-9.435934424e-01f,-9.435934424e-01f,-3.311063051e-01f,-3.311063051e-01f,9.435934424e-01f,9.435934424e-01f,3.311063051e-01f,
-2.755718231e-01f,-9.612804651e-01f,-4.848692417e-01f,8.745866418e-01f,9.612804651e-01f,-2.755718231e-01f,-8.745866418e-01f,-4.848692417e-01f,
5.000000000e-01f,5.000000000e-01f,5.000000000e-01f,-5.000000000e-01f,5.000000000e-01f,5.000000000e-01f,-7.883464098e-01f,-6.152315736e-01f,
8.140363097e-01f,-5.808139443e-01f,1.649131179e-01f,-9.863080978e-01f,-5.808139443e-01f,-8.140363097e-01f,-9.863080978e-01f,-1.649131179e-01f,
3.253102899e-01f,-9.456073046e-01f,-9.456073046e-01f,-3.253102899e-01f,-3.253102899e-01f,9.456073046e-01f,9.456073046e-01f,3.253102899e-01f,
-2.844075263e-01f,-9.587034583e-01f,-4.767992198e-01f,8.790122271e-01f,9.587034583e-01f,-2.844075263e-01f,-8.790122271e-01f,-4.767992198e-01f,
5.000000000e-01f,5.000000000e-01f,5.000000000e-01f,-5.000000000e-01f,5.000000000e-01f,5.000000000e-01f,-7.958369255e-01f,-6.055110693e-01f,
8.122506142e-01f,-5.833086371e-01f,1.618863940e-01f,-9.868093729e-01f,-5.833086371e-01f,-8.122506142e-01f,-9.868093729e-01f,-1.618863940e-01f,
3.195020258e-01f,-9.475855827e-01f,-9.475855827e-01f,-3.195020258e-01f,-3.195020258e-01f,9.475855827e-01f,9.475855827e-01f,3.195020258e-01f,
-2.932191491e-01f,-9.560452700e-01f,-4.686888158e-01f,8.833633661e-01f,9.560452700e-01f,-2.932191491e-01f,-8.833633661e-01f,-4.686888158e-01f,
5.000000000e-01f,5.000000000e-01f,5.000000000e-01f,-5.000000000e-01f,5.000000000e-01f,5.000000000e-01f,-8.032075167e-01f,-5.956993103e-01f,
8.104571700e-01f,-5.857978463e-01f,1.588581502e-01f,-9.873014092e-01f,-5.857978463e-01f,-8.104571700e-01f,-9.873014092e-01f,-1.588581502e-01f,
3.136817515e-01f,-9.495281577e-01f,-9.495281577e-01f,-3.136817515e-01f,-3.136817515e-01f,9.495281577e-01f,9.495281577e-01f,3.136817515e-01f,
-3.020059466e-01f,-9.533060193e-01f,-4.605387151e-01f,8.876396418e-01f,9.533060193e-01f,-3.020059466e-01f,-8.876396418e-01f,-4.605387151e-01f,
5.000000000e-01f,5.000000000e-01f,5.000000000e-01f,-5.000000000e-01f,5.000000000e-01f,5.000000000e-01f,-8.104571700e-01f,-5.857978463e-01f,
8.086561561e-01f,-5.882815719e-01f,1.558284014e-01f,-9.877841473e-01f,-5.882815719e-01f,-8.086561561e-01f,-9.877841473e-01f,-1.558284014e-01f,
3.078496456e-01f,-9.514350295e-01f,-9.514350295e-01f,-3.078496456e-01f,-3.078496456e-01f,9.514350295e-01f,9.514350295e-01f,3.078496456e-01f,
-3.107671440e-01f,-9.504860640e-01f,-4.523495734e-01f,8.918406963e-01f,9.504860640e-01f,-3.107671440e-01f,-8.918406963e-01f,-4.523495734e-01f,
5.000000000e-01f,5.000000000e-01f,5.000000000e-01f,-5.000000000e-01f,5.000000000e-01f,5.000000000e-01f,-8.175848126e-01f,-5.758081675e-01f,
8.068475723e-01f,-5.907596946e-01f,1.527971923e-01f,-9.882575870e-01f,-5.907596946e-01f,-8.068475723e-01f,-9.882575870e-01f,-1.527971923e-01f,
3.020059466e-01f,-9.533060193e-01f,-9.533060193e-01f,-3.020059466e-01f,-3.020059466e-01f,9.533060193e-01f,9.533060193e-01f,3.020059466e-01f,
-3.195020258e-01f,-9.475855827e-01f,-4.441221356e-01f,8.959662318e-01f,9.475855827e-01f,-3.195020258e-01f,-8.959662318e-01f,-4.441221356e-01f,
5.000000000e-01f,5.000000000e-01f,5.000000000e-01f,-5.000000000e-01f,5.000000000e-01f,5.000000000e-01f,-8.245893121e-01f,-5.657318234e-01f,
8.050313592e-01f,-5.932322741e-01f,1.497645378e-01f,-9.887216687e-01f,-5.932322741e-01f,-8.050313592e-01f,-9.887216687e-01f,-1.497645378e-01f,
2.961508930e-01f,-9.551411867e-01f,-9.551411867e-01f,-2.961508930e-01f,-2.961508930e-01f,9.551411867e-01f,9.551411867e-01f,2.961508930e-01f,
-3.282098472e-01f,-9.446048141e-01f,-4.358570874e-01f,9.000158906e-01f,9.446048141e-01f,-3.282098472e-01f,-9.000158906e-01f,-4.358570874e-01f,
5.000000000e-01f,5.000000000e-01f,5.000000000e-01f,-5.000000000e-01f,5.000000000e-01f,5.000000000e-01f,-8.314695954e-01f,-5.555702448e-01f,
8.032075167e-01f,-5.956993103e-01f,1.467304677e-01f,-9.891765118e-01f,-5.956993103e-01f,-8.032075167e-01f,-9.891765118e-01f,-1.467304677e-01f,
2.902846634e-01f,-9.569403529e-01f,-9.569403529e-01f,-2.902846634e-01f,-2.902846634e-01f,9.569403529e-01f,9.569403529e-01f,2.902846634e-01f,
-3.368898630e-01f,-9.415440559e-01f,-4.275550842e-01f,9.039893150e-01f,9.415440559e-01f,-3.368898630e-01f,-9.039893150e-01f,-4.275550842e-01f,
5.000000000e-01f,5.000000000e-01f,5.000000000e-01f,-5.000000000e-01f,5.000000000e-01f,5.000000000e-01f,-8.382247090e-01f,-5.453249812e-01f,
8.013761640e-01f,-5.981606841e-01f,1.436950266e-01f,-9.896219969e-01f,-5.981606841e-01f,-8.013761640e-01f,-9.896219969e-01f,-1.436950266e-01f,
2.844075263e-01f,-9.587034583e-01f,-9.587034583e-01f,-2.844075263e-01f,-2.844075263e-01f,9.587034583e-01f,9.587034583e-01f,2.844075263e-01f,
-3.455413282e-01f,-9.384035468e-01f,-4.192169011e-01f,9.078860879e-01f,9.384035468e-01f,-3.455413282e-01f,-9.078860879e-01f,-4.192169011e-01f,
5.000000000e-01f,5.000000000e-01f,5.000000000e-01f,-5.000000000e-01f,5.000000000e-01f,5.000000000e-01f,-8.448535800e-01f,-5.349976420e-01f,
7.995372415e-01f,-6.006164551e-01f,1.406582445e-01f,-9.900581837e-01f,-6.006164551e-01f,-7.995372415e-01f,-9.900581837e-01f,-1.406582445e-01f,
2.785196900e-01f,-9.604305029e-01f,-9.604305029e-01f,-2.785196900e-01f,-2.785196900e-01f,9.604305029e-01f,9.604305029e-01f,2.785196900e-01f,
-3.541635275e-01f,-9.351835251e-01f,-4.108431637e-01f,9.117060304e-01f,9.351835251e-01f,-3.541635275e-01f,-9.117060304e-01f,-4.108431637e-01f,
5.000000000e-01f,5.000000000e-01f,5.000000000e-01f,-5.000000000e-01f,5.000000000e-01f,5.000000000e-01f,-8.513551950e-01f,-5.245896578e-01f,
7.976908684e-01f,-6.030666232e-01f,1.376201212e-01f,-9.904850721e-01f,-6.030666232e-01f,-7.976908684e-01f,-9.904850721e-01f,-1.376201212e-01f,
2.726213634e-01f,-9.621214271e-01f,-9.621214271e-01f,-2.726213634e-01f,-2.726213634e-01f,9.621214271e-01f,9.621214271e-01f,2.726213634e-01f,
-3.627557158e-01f,-9.318842888e-01f,-4.024346471e-01f,9.154487252e-01f,9.318842888e-01f,-3.627557158e-01f,-9.154487252e-01f,-4.024346471e-01f,
5.000000000e-01f,5.000000000e-01f,5.000000000e-01f,-5.000000000e-01f,5.000000000e-01f,5.000000000e-01f,-8.577286005e-01f,-5.141027570e-01f,
7.958369255e-01f,-6.055110693e-01f,1.345807016e-01f,-9.909026623e-01f,-6.055110693e-01f,-7.958369255e-01f,-9.909026623e-01f,-1.345807016e-01f,
2.667127550e-01f,-9.637760520e-01f,-9.637760520e-01f,-2.667127550e-01f,-2.667127550e-01f,9.637760520e-01f,9.637760520e-01f,2.667127550e-01f,
-3.713172078e-01f,-9.285060763e-01f,-3.939920366e-01f,9.191138744e-01f,9.285060763e-01f,-3.713172078e-01f,-9.191138744e-01f,-3.939920366e-01f,
5.000000000e-01f,5.000000000e-01f,5.000000000e-01f,-5.000000000e-01f,5.000000000e-01f,5.000000000e-01f,-8.639728427e-01f,-5.035383701e-01f,
7.939754725e-01f,-6.079497933e-01f,1.315400302e-01f,-9.913108349e-01f,-6.079497933e-01f,-7.939754725e-01f,-9.913108349e-01f,-1.315400302e-01f,
2.607941031e-01f,-9.653944373e-01f,-9.653944373e-01f,-2.607941031e-01f,-2.607941031e-01f,9.653944373e-01f,9.653944373e-01f,2.607941031e-01f,
-3.798471987e-01f,-9.250492454e-01f,-3.855160475e-01f,9.227011204e-01f,9.250492454e-01f,-3.798471987e-01f,-9.227011204e-01f,-3.855160475e-01f,
5.000000000e-01f,5.000000000e-01f,5.000000000e-01f,-5.000000000e-01f,5.000000000e-01f,5.000000000e-01f,-8.700869679e-01f,-4.928981960e-01f,
7.921065688e-01f,-6.103827953e-01f,1.284981072e-01f,-9.917097688e-01f,-6.103827953e-01f,-7.921065688e-01f,-9.917097688e-01f,-1.284981072e-01f,
2.548656464e-01f,-9.669764638e-01f,-9.669764638e-01f,-2.548656464e-01f,-2.548656464e-01f,9.669764638e-01f,9.669764638e-01f,2.548656464e-01f,
-3.883450329e-01f,-9.215140343e-01f,-3.770074248e-01f,9.262102246e-01f,9.215140343e-01f,-3.883450329e-01f,-9.262102246e-01f,-3.770074248e-01f,
5.000000000e-01f,5.000000000e-01f,5.000000000e-01f,-5.000000000e-01f,5.000000000e-01f,5.000000000e-01f,-8.760700822e-01f,-4.821837842e-01f,
7.902302146e-01f,-6.128100753e-01f,1.254549772e-01f,-9.920992851e-01f,-6.128100753e-01f,-7.902302146e-01f,-9.920992851e-01f,-1.254549772e-01f,
2.489276081e-01f,-9.685220718e-01f,-9.685220718e-01f,-2.489276081e-01f,-2.489276081e-01f,9.685220718e-01f,9.685220718e-01f,2.489276081e-01f,
-3.968099952e-01f,-9.179008007e-01f,-3.684668243e-01f,9.296408892e-01f,9.179008007e-01f,-3.968099952e-01f,-9.296408892e-01f,-3.684668243e-01f,
5.000000000e-01f,5.000000000e-01f,5.000000000e-01f,-5.000000000e-01f,5.000000000e-01f,5.000000000e-01f,-8.819212914e-01f,-4.713967443e-01f,
7.883464098e-01f,-6.152315736e-01f,1.224106774e-01f,-9.924795628e-01f,-6.152315736e-01f,-7.883464098e-01f,-9.924795628e-01f,-1.224106774e-01f,
2.429801822e-01f,-9.700312614e-01f,-9.700312614e-01f,-2.429801822e-01f,-2.429801822e-01f,9.700312614e-01f,9.700312614e-01f,2.429801822e-01f,
-4.052413106e-01f,-9.142097831e-01f,-3.598950505e-01f,9.329928160e-01f,9.142097831e-01f,-4.052413106e-01f,-9.329928160e-01f,-3.598950505e-01f,
5.000000000e-01f,5.000000000e-01f,5.000000000e-01f,-5.000000000e-01f,5.000000000e-01f,5.000000000e-01f,-8.876396418e-01f,-4.605387151e-01f,
7.864552140e-01f,-6.176472902e-01f,1.193652153e-01f,-9.928504229e-01f,-6.176472902e-01f,-7.864552140e-01f,-9.928504229e-01f,-1.193652153e-01f,
2.370236069e-01f,-9.715039134e-01f,-9.715039134e-01f,-2.370236069e-01f,-2.370236069e-01f,9.715039134e-01f,9.715039134e-01f,2.370236069e-01f,
-4.136383235e-01f,-9.104412794e-01f,-3.512927592e-01f,9.362656474e-01f,9.104412794e-01f,-4.136383235e-01f,-9.362656474e-01f,-3.512927592e-01f,
5.000000000e-01f,5.000000000e-01f,5.000000000e-01f,-5.000000000e-01f,5.000000000e-01f,5.000000000e-01f,-8.932242990e-01f,-4.496113360e-01f,
7.845565677e-01f,-6.200572252e-01f,1.163186282e-01f,-9.932119250e-01f,-6.200572252e-01f,-7.845565677e-01f,-9.932119250e-01f,-1.163186282e-01f,
2.310581058e-01f,-9.729399681e-01f,-9.729399681e-01f,-2.310581058e-01f,-2.310581058e-01f,9.729399681e-01f,9.729399681e-01f,2.310581058e-01f,
-4.220002592e-01f,-9.065957069e-01f,-3.426607251e-01f,9.394592047e-01f,9.065957069e-01f,-4.220002592e-01f,-9.394592047e-01f,-3.426607251e-01f,
5.000000000e-01f,5.000000000e-01f,5.000000000e-01f,-5.000000000e-01f,5.000000000e-01f,5.000000000e-01f,-8.986744881e-01f,-4.386162460e-01f,
7.826505899e-01f,-6.224612594e-01f,1.132709533e-01f,-9.935641289e-01f,-6.224612594e-01f,-7.826505899e-01f,-9.935641289e-01f,-1.132709533e-01f,
2.250839174e-01f,-9.743393660e-01f,-9.743393660e-01f,-2.250839174e-01f,-2.250839174e-01f,9.743393660e-01f,9.743393660e-01f,2.250839174e-01f,
-4.303264916e-01f,-9.026733041e-01f,-3.339996636e-01f,9.425731897e-01f,9.026733041e-01f,-4.303264916e-01f,-9.425731897e-01f,-3.339996636e-01f,
5.000000000e-01f,5.000000000e-01f,5.000000000e-01f,-5.000000000e-01f,5.000000000e-01f,5.000000000e-01f,-9.039893150e-01f,-4.275550842e-01f,
7.807372212e-01f,-6.248595119e-01f,1.102222055e-01f,-9.939069748e-01f,-6.248595119e-01f,-7.807372212e-01f,-9.939069748e-01f,-1.102222055e-01f,
2.191012353e-01f,-9.757021070e-01f,-9.757021070e-01f,-2.191012353e-01f,-2.191012353e-01f,9.757021070e-01f,9.757021070e-01f,2.191012353e-01f,
-4.386162460e-01f,-8.986744881e-01f,-3.253102899e-01f,9.456073046e-01f,8.986744881e-01f,-4.386162460e-01f,-9.456073046e-01f,-3.253102899e-01f,
5.000000000e-01f,5.000000000e-01f,5.000000000e-01f,-5.000000000e-01f,5.000000000e-01f,5.000000000e-01f,-9.091680050e-01f,-4.164295495e-01f,
7.788165212e-01f,-6.272518039e-01f,1.071724221e-01f,-9.942404628e-01f,-6.272518039e-01f,-7.788165212e-01f,-9.942404628e-01f,-1.071724221e-01f,
2.131103128e-01f,-9.770281315e-01f,-9.770281315e-01f,-2.131103128e-01f,-2.131103128e-01f,9.770281315e-01f,9.770281315e-01f,2.131103128e-01f,
-4.468688369e-01f,-8.945994973e-01f,-3.165933788e-01f,9.485613704e-01f,8.945994973e-01f,-4.468688369e-01f,-9.485613704e-01f,-3.165933788e-01f,
5.000000000e-01f,5.000000000e-01f,5.000000000e-01f,-5.000000000e-01f,5.000000000e-01f,5.000000000e-01f,-9.142097831e-01f,-4.052413106e-01f,
7.768884897e-01f,-6.296382546e-01f,1.041216329e-01f,-9.945645928e-01f,-6.296382546e-01f,-7.768884897e-01f,-9.945645928e-01f,-1.041216329e-01f,
2.071113735e-01f,-9.783173800e-01f,-9.783173800e-01f,-2.071113735e-01f,-2.071113735e-01f,9.783173800e-01f,9.783173800e-01f,2.071113735e-01f,
-4.550835788e-01f,-8.904487491e-01f,-3.078496456e-01f,9.514350295e-01f,8.904487491e-01f,-4.550835788e-01f,-9.514350295e-01f,-3.078496456e-01f,
5.000000000e-01f,5.000000000e-01f,5.000000000e-01f,-5.000000000e-01f,5.000000000e-01f,5.000000000e-01f,-9.191138744e-01f,-3.939920366e-01f,
7.749531269e-01f,-6.320187449e-01f,1.010698602e-01f,-9.948793054e-01f,-6.320187449e-01f,-7.749531269e-01f,-9.948793054e-01f,-1.010698602e-01f,
2.011046410e-01f,-9.795697927e-01f,-9.795697927e-01f,-2.011046410e-01f,-2.011046410e-01f,9.795697927e-01f,9.795697927e-01f,2.011046410e-01f,
-4.632597864e-01f,-8.862225413e-01f,-2.990798354e-01f,9.542281032e-01f,8.862225413e-01f,-4.632597864e-01f,-9.542281032e-01f,-2.990798354e-01f,
5.000000000e-01f,5.000000000e-01f,5.000000000e-01f,-5.000000000e-01f,5.000000000e-01f,5.000000000e-01f,-9.238795042e-01f,-3.826834261e-01f,
7.730104327e-01f,-6.343932748e-01f,9.801714122e-02f,-9.951847196e-01f,-6.343932748e-01f,-7.730104327e-01f,-9.951847196e-01f,-9.801714122e-02f,
1.950903237e-01f,-9.807852507e-01f,-9.807852507e-01f,-1.950903237e-01f,-1.950903237e-01f,9.807852507e-01f,9.807852507e-01f,1.950903237e-01f,
-4.713967443e-01f,-8.819212914e-01f,-2.902846634e-01f,9.569403529e-01f,8.819212914e-01f,-4.713967443e-01f,-9.569403529e-01f,-2.902846634e-01f,
5.000000000e-01f,5.000000000e-01f,5.000000000e-01f,-5.000000000e-01f,5.000000000e-01f,5.000000000e-01f,-9.285060763e-01f,-3.713172078e-01f,
7.710605264e-01f,-6.367618442e-01f,9.496349841e-02f,-9.954807758e-01f,-6.367618442e-01f,-7.710605264e-01f,-9.954807758e-01f,-9.496349841e-02f,
1.890686601e-01f,-9.819638729e-01f,-9.819638729e-01f,-1.890686601e-01f,-1.890686601e-01f,9.819638729e-01f,9.819638729e-01f,1.890686601e-01f,
-4.794937670e-01f,-8.775452971e-01f,-2.814649343e-01f,9.595715404e-01f,8.775452971e-01f,-4.794937670e-01f,-9.595715404e-01f,-2.814649343e-01f,
5.000000000e-01f,5.000000000e-01f,5.000000000e-01f,-5.000000000e-01f,5.000000000e-01f,5.000000000e-01f,-9.329928160e-01f,-3.598950505e-01f,
7.691033483e-01f,-6.391244531e-01f,9.190895408e-02f,-9.957674146e-01f,-6.391244531e-01f,-7.691033483e-01f,-9.957674146e-01f,-9.190895408e-02f,
1.830398887e-01f,-9.831054807e-01f,-9.831054807e-01f,-1.830398887e-01f,-1.830398887e-01f,9.831054807e-01f,9.831054807e-01f,1.830398887e-01f,
-4.875501692e-01f,-8.730949759e-01f,-2.726213634e-01f,9.621214271e-01f,8.730949759e-01f,-4.875501692e-01f,-9.621214271e-01f,-2.726213634e-01f,
5.000000000e-01f,5.000000000e-01f,5.000000000e-01f,-5.000000000e-01f,5.000000000e-01f,5.000000000e-01f,-9.373390079e-01f,-3.484186828e-01f,
7.671388984e-01f,-6.414810419e-01f,8.885355294e-02f,-9.960446954e-01f,-6.414810419e-01f,-7.671388984e-01f,-9.960446954e-01f,-8.885355294e-02f,
1.770042181e-01f,-9.842100739e-01f,-9.842100739e-01f,-1.770042181e-01f,-1.770042181e-01f,9.842100739e-01f,9.842100739e-01f,1.770042181e-01f,
-4.955652654e-01f,-8.685706854e-01f,-2.637546659e-01f,9.645897746e-01f,8.685706854e-01f,-4.955652654e-01f,-9.645897746e-01f,-2.637546659e-01f,
5.000000000e-01f,5.000000000e-01f,5.000000000e-01f,-5.000000000e-01f,5.000000000e-01f,5.000000000e-01f,-9.415440559e-01f,-3.368898630e-01f,
7.651672363e-01f,-6.438315511e-01f,8.579730988e-02f,-9.963126183e-01f,-6.438315511e-01f,-7.651672363e-01f,-9.963126183e-01f,-8.579730988e-02f,
1.709618866e-01f,-9.852776527e-01f,-9.852776527e-01f,-1.709618866e-01f,-1.709618866e-01f,9.852776527e-01f,9.852776527e-01f,1.709618866e-01f,
-5.035383701e-01f,-8.639728427e-01f,-2.548656464e-01f,9.669764638e-01f,8.639728427e-01f,-5.035383701e-01f,-9.669764638e-01f,-2.548656464e-01f,
5.000000000e-01f,5.000000000e-01f,5.000000000e-01f,-5.000000000e-01f,5.000000000e-01f,5.000000000e-01f,-9.456073046e-01f,-3.253102899e-01f,
7.631884217e-01f,-6.461760402e-01f,8.274026215e-02f,-9.965711236e-01f,-6.461760402e-01f,-7.631884217e-01f,-9.965711236e-01f,-8.274026215e-02f,
1.649131179e-01f,-9.863080978e-01f,-9.863080978e-01f,-1.649131179e-01f,-1.649131179e-01f,9.863080978e-01f,9.863080978e-01f,1.649131179e-01f,
-5.114688277e-01f,-8.593018055e-01f,-2.459550500e-01f,9.692812562e-01f,8.593018055e-01f,-5.114688277e-01f,-9.692812562e-01f,-2.459550500e-01f,
5.000000000e-01f,5.000000000e-01f,5.000000000e-01f,-5.000000000e-01f,5.000000000e-01f,5.000000000e-01f,-9.495281577e-01f,-3.136817515e-01f,
7.612023950e-01f,-6.485143900e-01f,7.968243957e-02f,-9.968202710e-01f,-6.485143900e-01f,-7.612023950e-01f,-9.968202710e-01f,-7.968243957e-02f,
1.588581502e-01f,-9.873014092e-01f,-9.873014092e-01f,-1.588581502e-01f,-1.588581502e-01f,9.873014092e-01f,9.873014092e-01f,1.588581502e-01f,
-5.193560123e-01f,-8.545579910e-01f,-2.370236069e-01f,9.715039134e-01f,8.545579910e-01f,-5.193560123e-01f,-9.715039134e-01f,-2.370236069e-01f,
5.000000000e-01f,5.000000000e-01f,5.000000000e-01f,-5.000000000e-01f,5.000000000e-01f,5.000000000e-01f,-9.533060193e-01f,-3.020059466e-01f,
7.592092156e-01f,-6.508466601e-01f,7.662386447e-02f,-9.970600605e-01f,-6.508466601e-01f,-7.592092156e-01f,-9.970600605e-01f,-7.662386447e-02f,
1.527971923e-01f,-9.882575870e-01f,-9.882575870e-01f,-1.527971923e-01f,-1.527971923e-01f,9.882575870e-01f,9.882575870e-01f,1.527971923e-01f,
-5.271991491e-01f,-8.497417569e-01f,-2.280720770e-01f,9.736442566e-01f,8.497417569e-01f,-5.271991491e-01f,-9.736442566e-01f,-2.280720770e-01f,
5.000000000e-01f,5.000000000e-01f,5.000000000e-01f,-5.000000000e-01f,5.000000000e-01f,5.000000000e-01f,-9.569403529e-01f,-2.902846634e-01f,
7.572088242e-01f,-6.531728506e-01f,7.356456667e-02f,-9.972904325e-01f,-6.531728506e-01f,-7.572088242e-01f,-9.972904325e-01f,-7.356456667e-02f,
1.467304677e-01f,-9.891765118e-01f,-9.891765118e-01f,-1.467304677e-01f,-1.467304677e-01f,9.891765118e-01f,9.891765118e-01f,1.467304677e-01f,
-5.349976420e-01f,-8.448535800e-01f,-2.191012353e-01f,9.757021070e-01f,8.448535800e-01f,-5.349976420e-01f,-9.757021070e-01f,-2.191012353e-01f,
5.000000000e-01f,5.000000000e-01f,5.000000000e-01f,-5.000000000e-01f,5.000000000e-01f,5.000000000e-01f,-9.604305029e-01f,-2.785196900e-01f,
7.552013993e-01f,-6.554928422e-01f,7.050457597e-02f,-9.975114465e-01f,-6.554928422e-01f,-7.552013993e-01f,-9.975114465e-01f,-7.050457597e-02f,
1.406582445e-01f,-9.900581837e-01f,-9.900581837e-01f,-1.406582445e-01f,-1.406582445e-01f,9.900581837e-01f,9.900581837e-01f,1.406582445e-01f,
-5.427507758e-01f,-8.398938179e-01f,-2.101118416e-01f,9.776773453e-01f,8.398938179e-01f,-5.427507758e-01f,-9.776773453e-01f,-2.101118416e-01f,
5.000000000e-01f,5.000000000e-01f,5.000000000e-01f,-5.000000000e-01f,5.000000000e-01f,5.000000000e-01f,-9.637760520e-01f,-2.667127550e-01f,
7.531868219e-01f,-6.578066945e-01f,6.744392216e-02f,-9.977230430e-01f,-6.578066945e-01f,-7.531868219e-01f,-9.977230430e-01f,-6.744392216e-02f,
1.345807016e-01f,-9.909026623e-01f,-9.909026623e-01f,-1.345807016e-01f,-1.345807016e-01f,9.909026623e-01f,9.909026623e-01f,1.345807016e-01f,
-5.504579544e-01f,-8.348628879e-01f,-2.011046410e-01f,9.795697927e-01f,8.348628879e-01f,-5.504579544e-01f,-9.795697927e-01f,-2.011046410e-01f,
5.000000000e-01f,5.000000000e-01f,5.000000000e-01f,-5.000000000e-01f,5.000000000e-01f,5.000000000e-01f,-9.669764638e-01f,-2.548656464e-01f,
7.511651516e-01f,-6.601143479e-01f,6.438262761e-02f,-9.979252815e-01f,-6.601143479e-01f,-7.511651516e-01f,-9.979252815e-01f,-6.438262761e-02f,
1.284981072e-01f,-9.917097688e-01f,-9.917097688e-01f,-1.284981072e-01f,-1.284981072e-01f,9.917097688e-01f,9.917097688e-01f,1.284981072e-01f,
-5.581185222e-01f,-8.297612071e-01f,-1.920803934e-01f,9.813792109e-01f,8.297612071e-01f,-5.581185222e-01f,-9.813792109e-01f,-1.920803934e-01f,
5.000000000e-01f,5.000000000e-01f,5.000000000e-01f,-5.000000000e-01f,5.000000000e-01f,5.000000000e-01f,-9.700312614e-01f,-2.429801822e-01f,
7.491363883e-01f,-6.624158025e-01f,6.132073700e-02f,-9.981181026e-01f,-6.624158025e-01f,-7.491363883e-01f,-9.981181026e-01f,-6.132073700e-02f,
1.224106774e-01f,-9.924795628e-01f,-9.924795628e-01f,-1.224106774e-01f,-1.224106774e-01f,9.924795628e-01f,9.924795628e-01f,1.224106774e-01f,
-5.657318234e-01f,-8.245893121e-01f,-1.830398887e-01f,9.831054807e-01f,8.245893121e-01f,-5.657318234e-01f,-9.831054807e-01f,-1.830398887e-01f,
5.000000000e-01f,5.000000000e-01f,5.000000000e-01f,-5.000000000e-01f,5.000000000e-01f,5.000000000e-01f,-9.729399681e-01f,-2.310581058e-01f,
7.471005917e-01f,-6.647109985e-01f,5.825826526e-02f,-9.983015656e-01f,-6.647109985e-01f,-7.471005917e-01f,-9.983015656e-01f,-5.825826526e-02f,
1.163186282e-01f,-9.932119250e-01f,-9.932119250e-01f,-1.163186282e-01f,-1.163186282e-01f,9.932119250e-01f,9.932119250e-01f,1.163186282e-01f,
-5.732971430e-01f,-8.193475008e-01f,-1.739838719e-01f,9.847484827e-01f,8.193475008e-01f,-5.732971430e-01f,-9.847484827e-01f,-1.739838719e-01f,
5.000000000e-01f,5.000000000e-01f,5.000000000e-01f,-5.000000000e-01f,5.000000000e-01f,5.000000000e-01f,-9.757021070e-01f,-2.191012353e-01f,
7.450577617e-01f,-6.669999361e-01f,5.519524589e-02f,-9.984755516e-01f,-6.669999361e-01f,-7.450577617e-01f,-9.984755516e-01f,-5.519524589e-02f,
1.102222055e-01f,-9.939069748e-01f,-9.939069748e-01f,-1.102222055e-01f,-1.102222055e-01f,9.939069748e-01f,9.939069748e-01f,1.102222055e-01f,
-5.808139443e-01f,-8.140363097e-01f,-1.649131179e-01f,9.863080978e-01f,8.140363097e-01f,-5.808139443e-01f,-9.863080978e-01f,-1.649131179e-01f,
5.000000000e-01f,5.000000000e-01f,5.000000000e-01f,-5.000000000e-01f,5.000000000e-01f,5.000000000e-01f,-9.783173800e-01f,-2.071113735e-01f,
7.430079579e-01f,-6.692826152e-01f,5.213170499e-02f,-9.986402392e-01f,-6.692826152e-01f,-7.430079579e-01f,-9.986402392e-01f,-5.213170499e-02f,
1.041216329e-01f,-9.945645928e-01f,-9.945645928e-01f,-1.041216329e-01f,-1.041216329e-01f,9.945645928e-01f,9.945645928e-01f,1.041216329e-01f,
-5.882815719e-01f,-8.086561561e-01f,-1.558284014e-01f,9.877841473e-01f,8.086561561e-01f,-5.882815719e-01f,-9.877841473e-01f,-1.558284014e-01f,
5.000000000e-01f,5.000000000e-01f,5.000000000e-01f,-5.000000000e-01f,5.000000000e-01f,5.000000000e-01f,-9.807852507e-01f,-1.950903237e-01f,
7.409511209e-01f,-6.715589762e-01f,4.906767607e-02f,-9.987954497e-01f,-6.715589762e-01f,-7.409511209e-01f,-9.987954497e-01f,-4.906767607e-02f,
9.801714122e-02f,-9.951847196e-01f,-9.951847196e-01f,-9.801714122e-02f,-9.801714122e-02f,9.951847196e-01f,9.951847196e-01f,9.801714122e-02f,
-5.956993103e-01f,-8.032075167e-01f,-1.467304677e-01f,9.891765118e-01f,8.032075167e-01f,-5.956993103e-01f,-9.891765118e-01f,-1.467304677e-01f,
5.000000000e-01f,5.000000000e-01f,5.000000000e-01f,-5.000000000e-01f,5.000000000e-01f,5.000000000e-01f,-9.831054807e-01f,-1.830398887e-01f,
7.388873100e-01f,-6.738290191e-01f,4.600318149e-02f,-9.989413023e-01f,-6.738290191e-01f,-7.388873100e-01f,-9.989413023e-01f,-4.600318149e-02f,
9.190895408e-02f,-9.957674146e-01f,-9.957674146e-01f,-9.190895408e-02f,-9.190895408e-02f,9.957674146e-01f,9.957674146e-01f,9.190895408e-02f,
-6.030666232e-01f,-7.976908684e-01f,-1.376201212e-01f,9.904850721e-01f,7.976908684e-01f,-6.030666232e-01f,-9.904850721e-01f,-1.376201212e-01f,
5.000000000e-01f,5.000000000e-01f,5.000000000e-01f,-5.000000000e-01f,5.000000000e-01f,5.000000000e-01f,-9.852776527e-01f,-1.709618866e-01f,
7.368165851e-01f,-6.760926843e-01f,4.293825850e-02f,-9.990777373e-01f,-6.760926843e-01f,-7.368165851e-01f,-9.990777373e-01f,-4.293825850e-02f,
8.579730988e-02f,-9.963126183e-01f,-9.963126183e-01f,-8.579730988e-02f,-8.579730988e-02f,9.963126183e-01f,9.963126183e-01f,8.579730988e-02f,
-6.103827953e-01f,-7.921065688e-01f,-1.284981072e-01f,9.917097688e-01f,7.921065688e-01f,-6.103827953e-01f,-9.917097688e-01f,-1.284981072e-01f,
5.000000000e-01f,5.000000000e-01f,5.000000000e-01f,-5.000000000e-01f,5.000000000e-01f,5.000000000e-01f,-9.873014092e-01f,-1.588581502e-01f,
7.347388864e-01f,-6.783500314e-01f,3.987292573e-02f,-9.992047548e-01f,-6.783500314e-01f,-7.347388864e-01f,-9.992047548e-01f,-3.987292573e-02f,
7.968243957e-02f,-9.968202710e-01f,-9.968202710e-01f,-7.968243957e-02f,-7.968243957e-02f,9.968202710e-01f,9.968202710e-01f,7.968243957e-02f,
-6.176472902e-01f,-7.864552140e-01f,-1.193652153e-01f,9.928504229e-01f,7.864552140e-01f,-6.176472902e-01f,-9.928504229e-01f,-1.193652153e-01f,
5.000000000e-01f,5.000000000e-01f,5.000000000e-01f,-5.000000000e-01f,5.000000000e-01f,5.000000000e-01f,-9.891765118e-01f,-1.467304677e-01f,
7.326542735e-01f,-6.806010008e-01f,3.680722415e-02f,-9.993223548e-01f,-6.806010008e-01f,-7.326542735e-01f,-9.993223548e-01f,-3.680722415e-02f,
7.356456667e-02f,-9.972904325e-01f,-9.972904325e-01f,-7.356456667e-02f,-7.356456667e-02f,9.972904325e-01f,9.972904325e-01f,7.356456667e-02f,
-6.248595119e-01f,-7.807372212e-01f,-1.102222055e-01f,9.939069748e-01f,7.807372212e-01f,-6.248595119e-01f,-9.939069748e-01f,-1.102222055e-01f,
5.000000000e-01f,5.000000000e-01f,5.000000000e-01f,-5.000000000e-01f,5.000000000e-01f,5.000000000e-01f,-9.909026623e-01f,-1.345807016e-01f,
7.305627465e-01f,-6.828455329e-01f,3.374117240e-02f,-9.994305968e-01f,-6.828455329e-01f,-7.305627465e-01f,-9.994305968e-01f,-3.374117240e-02f,
6.744392216e-02f,-9.977230430e-01f,-9.977230430e-01f,-6.744392216e-02f,-6.744392216e-02f,9.977230430e-01f,9.977230430e-01f,6.744392216e-02f,
-6.320187449e-01f,-7.749531269e-01f,-1.010698602e-01f,9.948793054e-01f,7.749531269e-01f,-6.320187449e-01f,-9.948793054e-01f,-1.010698602e-01f,
5.000000000e-01f,5.000000000e-01f,5.000000000e-01f,-5.000000000e-01f,5.000000000e-01f,5.000000000e-01f,-9.924795628e-01f,-1.224106774e-01f,
7.284643650e-01f,-6.850836873e-01f,3.067480400e-02f,-9.995294213e-01f,-6.850836873e-01f,-7.284643650e-01f,-9.995294213e-01f,-3.067480400e-02f,
6.132073700e-02f,-9.981181026e-01f,-9.981181026e-01f,-6.132073700e-02f,-6.132073700e-02f,9.981181026e-01f,9.981181026e-01f,6.132073700e-02f,
-6.391244531e-01f,-7.691033483e-01f,-9.190895408e-02f,9.957674146e-01f,7.691033483e-01f,-6.391244531e-01f,-9.957674146e-01f,-9.190895408e-02f,
5.000000000e-01f,5.000000000e-01f,5.000000000e-01f,-5.000000000e-01f,5.000000000e-01f,5.000000000e-01f,-9.939069748e-01f,-1.102222055e-01f,
7.263591290e-01f,-6.873153448e-01f,2.760814503e-02f,-9.996188283e-01f,-6.873153448e-01f,-7.263591290e-01f,-9.996188283e-01f,-2.760814503e-02f,
5.519524589e-02f,-9.984755516e-01f,-9.984755516e-01f,-5.519524589e-02f,-5.519524589e-02f,9.984755516e-01f,9.984755516e-01f,5.519524589e-02f,
-6.461760402e-01f,-7.631884217e-01f,-8.274026215e-02f,9.965711236e-01f,7.631884217e-01f,-6.461760402e-01f,-9.965711236e-01f,-8.274026215e-02f,
5.000000000e-01f,5.000000000e-01f,5.000000000e-01f,-5.000000000e-01f,5.000000000e-01f,5.000000000e-01f,-9.951847196e-01f,-9.801714122e-02f,
7.242470980e-01f,-6.895405650e-01f,2.454122901e-02f,-9.996988177e-01f,-6.895405650e-01f,-7.242470980e-01f,-9.996988177e-01f,-2.454122901e-02f,
4.906767607e-02f,-9.987954497e-01f,-9.987954497e-01f,-4.906767607e-02f,-4.906767607e-02f,9.987954497e-01f,9.987954497e-01f,4.906767607e-02f,
-6.531728506e-01f,-7.572088242e-01f,-7.356456667e-02f,9.972904325e-01f,7.572088242e-01f,-6.531728506e-01f,-9.972904325e-01f,-7.356456667e-02f,
5.000000000e-01f,5.000000000e-01f,5.000000000e-01f,-5.000000000e-01f,5.000000000e-01f,5.000000000e-01f,-9.963126183e-01f,-8.579730988e-02f,
7.221282125e-01f,-6.917592287e-01f,2.147408016e-02f,-9.997693896e-01f,-6.917592287e-01f,-7.221282125e-01f,-9.997693896e-01f,-2.147408016e-02f,
4.293825850e-02f,-9.990777373e-01f,-9.990777373e-01f,-4.293825850e-02f,-4.293825850e-02f,9.990777373e-01f,9.990777373e-01f,4.293825850e-02f,
-6.601143479e-01f,-7.511651516e-01f,-6.438262761e-02f,9.979252815e-01f,7.511651516e-01f,-6.601143479e-01f,-9.979252815e-01f,-6.438262761e-02f,
5.000000000e-01f,5.000000000e-01f,5.000000000e-01f,-5.000000000e-01f,5.000000000e-01f,5.000000000e-01f,-9.972904325e-01f,-7.356456667e-02f,
7.200025320e-01f,-6.939714551e-01f,1.840673015e-02f,-9.998306036e-01f,-6.939714551e-01f,-7.200025320e-01f,-9.998306036e-01f,-1.840673015e-02f,
3.680722415e-02f,-9.993223548e-01f,-9.993223548e-01f,-3.680722415e-02f,-3.680722415e-02f,9.993223548e-01f,9.993223548e-01f,3.680722415e-02f,
-6.669999361e-01f,-7.450577617e-01f,-5.519524589e-02f,9.984755516e-01f,7.450577617e-01f,-6.669999361e-01f,-9.984755516e-01f,-5.519524589e-02f,
5.000000000e-01f,5.000000000e-01f,5.000000000e-01f,-5.000000000e-01f,5.000000000e-01f,5.000000000e-01f,-9.981181026e-01f,-6.132073700e-02f,
7.178700566e-01f,-6.961771250e-01f,1.533920597e-02f,-9.998823404e-01f,-6.961771250e-01f,-7.178700566e-01f,-9.998823404e-01f,-1.533920597e-02f,
3.067480400e-02f,-9.995294213e-01f,-9.995294213e-01f,-3.067480400e-02f,-3.067480400e-02f,9.995294213e-01f,9.995294213e-01f,3.067480400e-02f,
-6.738290191e-01f,-7.388873100e-01f,-4.600318149e-02f,9.989413023e-01f,7.388873100e-01f,-6.738290191e-01f,-9.989413023e-01f,-4.600318149e-02f,
5.000000000e-01f,5.000000000e-01f,5.000000000e-01f,-5.000000000e-01f,5.000000000e-01f,5.000000000e-01f,-9.987954497e-01f,-4.906767607e-02f,
7.157308459e-01f,-6.983762383e-01f,1.227153838e-02f,-9.999247193e-01f,-6.983762383e-01f,-7.157308459e-01f,-9.999247193e-01f,-1.227153838e-02f,
2.454122901e-02f,-9.996988177e-01f,-9.996988177e-01f,-2.454122901e-02f,-2.454122901e-02f,9.996988177e-01f,9.996988177e-01f,2.454122901e-02f,
-6.806010008e-01f,-7.326542735e-01f,-3.680722415e-02f,9.993223548e-01f,7.326542735e-01f,-6.806010008e-01f,-9.993223548e-01f,-3.680722415e-02f,
5.000000000e-01f,5.000000000e-01f,5.000000000e-01f,-5.000000000e-01f,5.000000000e-01f,5.000000000e-01f,-9.993223548e-01f,-3.680722415e-02f,
7.135848403e-01f,-7.005687952e-01f,9.203754365e-03f,-9.999576211e-01f,-7.005687952e-01f,-7.135848403e-01f,-9.999576211e-01f,-9.203754365e-03f,
1.840673015e-02f,-9.998306036e-01f,-9.998306036e-01f,-1.840673015e-02f,-1.840673015e-02f,9.998306036e-01f,9.998306036e-01f,1.840673015e-02f,
-6.873153448e-01f,-7.263591290e-01f,-2.760814503e-02f,9.996188283e-01f,7.263591290e-01f,-6.873153448e-01f,-9.996188283e-01f,-2.760814503e-02f,
5.000000000e-01f,5.000000000e-01f,5.000000000e-01f,-5.000000000e-01f,5.000000000e-01f,5.000000000e-01f,-9.996988177e-01f,-2.454122901e-02f,
7.114322186e-01f,-7.027547359e-01f,6.135884672e-03f,-9.999811649e-01f,-7.027547359e-01f,-7.114322186e-01f,-9.999811649e-01f,-6.135884672e-03f,
1.227153838e-02f,-9.999247193e-01f,-9.999247193e-01f,-1.227153838e-02f,-1.227153838e-02f,9.999247193e-01f,9.999247193e-01f,1.227153838e-02f,
-6.939714551e-01f,-7.200025320e-01f,-1.840673015e-02f,9.998306036e-01f,7.200025320e-01f,-6.939714551e-01f,-9.998306036e-01f,-1.840673015e-02f,
5.000000000e-01f,5.000000000e-01f,5.000000000e-01f,-5.000000000e-01f,5.000000000e-01f,5.000000000e-01f,-9.999247193e-01f,-1.227153838e-02f,
7.092728019e-01f,-7.049340606e-01f,3.067956772e-03f,-9.999952912e-01f,-7.049340606e-01f,-7.092728019e-01f,-9.999952912e-01f,-3.067956772e-03f,
6.135884672e-03f,-9.999811649e-01f,-9.999811649e-01f,-6.135884672e-03f,-6.135884672e-03f,9.999811649e-01f,9.999811649e-01f,6.135884672e-03f,
-7.005687952e-01f,-7.135848403e-01f,-9.203754365e-03f,9.999576211e-01f,7.135848403e-01f,-7.005687952e-01f,-9.999576211e-01f,-9.203754365e-03f};

#define DEVI __device__ __forceinline__

DEVI float bf2f(unsigned v) { return __uint_as_float(v << 16); }
DEVI float bfhi(unsigned v) { return __uint_as_float(v & 0xffff0000u); }
DEVI unsigned pk_bf16(c32 v) { return __builtin_bit_cast(unsigned, __builtin_convertvector(v, bf16x2_t)); }
DEVI c32 rbf2(c32 v) { unsigned u = pk_bf16(v); return c32{bf2f(u), bfhi(u)}; }
DEVI float rbf(float v) { return bf2f(pk_bf16(c32{v, v}) & 0xffffu); }
DEVI c32 fma2(c32 a, c32 b, c32 c) { return __builtin_elementwise_fma(a, b, c); }

DEVI c32 add_mi(c32 a, c32 b) { c32 r; asm("v_pk_add_f32 %0, %1, %2 op_sel:[0,1] op_sel_hi:[1,0] neg_hi:[0,1]" : "=v"(r) : "v"(a), "v"(b)); return r; }
DEVI c32 add_pi(c32 a, c32 b) { c32 r; asm("v_pk_add_f32 %0, %1, %2 op_sel:[0,1] op_sel_hi:[1,0] neg_lo:[0,1]" : "=v"(r) : "v"(a), "v"(b)); return r; }
DEVI c32 nsub_mi(c32 a) { c32 r; asm("v_pk_add_f32 %0, %1, %1 op_sel:[0,1] op_sel_hi:[1,0] neg_lo:[1,0] neg_hi:[1,1]" : "=v"(r) : "v"(a)); return r; }
DEVI c32 nsub_pi(c32 a) { c32 r; asm("v_pk_add_f32 %0, %1, %1 op_sel:[0,1] op_sel_hi:[1,0] neg_lo:[1,1] neg_hi:[1,0]" : "=v"(r) : "v"(a)); return r; }
DEVI c32 fma_mi(c32 d, c32 s, c32 c) { c32 r; asm("v_pk_fma_f32 %0, %1, %2, %3 op_sel:[1,0,0] op_sel_hi:[0,1,1] neg_hi:[1,0,0]" : "=v"(r) : "v"(d), "s"(s), "v"(c)); return r; }
DEVI c32 fma_pi(c32 d, c32 s, c32 c) { c32 r; asm("v_pk_fma_f32 %0, %1, %2, %3 op_sel:[1,0,0] op_sel_hi:[0,1,1] neg_lo:[1,0,0]" : "=v"(r) : "v"(d), "s"(s), "v"(c)); return r; }
DEVI c32 add_cj(c32 a, c32 b) { c32 r; asm("v_pk_add_f32 %0, %1, %2 neg_hi:[0,1]" : "=v"(r) : "v"(a), "v"(b)); return r; }
DEVI c32 sub_cj_mi(c32 a, c32 b) { c32 r; asm("v_pk_add_f32 %0, %1, %2 op_sel:[1,1] op_sel_hi:[0,0] neg_hi:[1,0]" : "=v"(r) : "v"(a), "v"(b)); return r; }
DEVI c32 cj_add_i(c32 a, c32 b) { c32 r; asm("v_pk_add_f32 %0, %1, %2 op_sel:[0,1] op_sel_hi:[1,0] neg_hi:[1,0]" : "=v"(r) : "v"(a), "v"(b)); return r; }
DEVI c32 sumdiff(c32 v) { c32 r; asm("v_pk_add_f32 %0, %1, %1 op_sel:[0,1] op_sel_hi:[0,1] neg_hi:[0,1]" : "=v"(r) : "v"(v)); return r; }

#define CMUL_DEF(NAME, WC, M1, M2) \
DEVI c32 NAME(c32 a, c32 w) { c32 r; \
    asm("v_pk_mul_f32 %0, %1, %2 op_sel:[1,1] op_sel_hi:[1,0]" M1 "\n\t" \
        "v_pk_fma_f32 %0, %1, %2, %0 op_sel:[0,0,0] op_sel_hi:[0,1,1]" M2 : "=&v"(r) : "v"(a), WC(w)); return r; }
CMUL_DEF(cmul, "v", " neg_lo:[0,1]", "")
CMUL_DEF(cmulc, "v", "", " neg_hi:[0,1,0]")
CMUL_DEF(cmul_s, "s", " neg_lo:[0,1]", "")
CMUL_DEF(cmulc_s, "s", "", " neg_hi:[0,1,0]")

template <int DIR> DEVI c32 tw(c32 a, c32 w) { if constexpr (DIR < 0) return cmul(a, w); else return cmulc(a, w); }
template <int DIR> DEVI c32 tws(c32 a, c32 w) { if constexpr (DIR < 0) return cmul_s(a, w); else return cmulc_s(a, w); }
template <int DIR> DEVI c32 addr(c32 a, c32 b) { if constexpr (DIR < 0) return add_mi(a, b); else return add_pi(a, b); }
template <int DIR> DEVI c32 subr(c32 a, c32 b) { if constexpr (DIR < 0) return add_pi(a, b); else return add_mi(a, b); }
template <int DIR> DEVI c32 nsubr(c32 a) { if constexpr (DIR < 0) return nsub_mi(a); else return nsub_pi(a); }
template <int DIR> DEVI c32 fmar(c32 d, c32 s, c32 c) { if constexpr (DIR < 0) return fma_mi(d, s, c); else return fma_pi(d, s, c); }
template <int DIR> DEVI c32 fmsr(c32 d, c32 s, c32 c) { if constexpr (DIR < 0) return fma_pi(d, s, c); else return fma_mi(d, s, c); }

template <int DIR> DEVI void r4(c32& x0, c32& x1, c32& x2, c32& x3) {
    c32 a0 = x0 + x2, a1 = x0 - x2, a2 = x1 + x3, a3 = x1 - x3;
    x0 = a0 + a2; x2 = a0 - a2; x1 = addr<DIR>(a1, a3); x3 = subr<DIR>(a1, a3);
}
template <int DIR> DEVI void r4w8(c32& x0, c32& x1, c32& x2, c32& x3) {
    constexpr float R = 0.70710678118654752440f;
    const c32 RR = {R, R};
    c32 u = addr<DIR>(x1, x1), v = nsubr<DIR>(x3);
    c32 s = u + v, d = u - v;
    c32 a0 = addr<DIR>(x0, x2), a1 = subr<DIR>(x0, x2);
    x0 = fma2(s, RR, a0); x2 = fma2(s, -RR, a0);
    x1 = fmar<DIR>(d, RR, a1); x3 = fmsr<DIR>(d, RR, a1);
}
template <int DIR> DEVI void r8(c32 (&x)[8]) {
    c32 a0 = x[0] + x[4], a1 = x[1] + x[5], a2 = x[2] + x[6], a3 = x[3] + x[7];
    c32 b0 = x[0] - x[4], b1 = x[1] - x[5], b2 = x[2] - x[6], b3 = x[3] - x[7];
    r4<DIR>(a0, a1, a2, a3);
    r4w8<DIR>(b0, b1, b2, b3);
    x[0] = a0; x[2] = a1; x[4] = a2; x[6] = a3;
    x[1] = b0; x[3] = b1; x[5] = b2; x[7] = b3;
}
template <int DIR> DEVI void r16(c32 (&x)[16]) {
    constexpr float C1 = 0.92387953251128675613f, S1 = 0.38268343236508977173f, R = 0.70710678118654752440f;
    const c32 w1 = {C1, -S1}, w3 = {S1, -C1}, w9 = {-C1, S1}, RR = {R, R};
#pragma unroll
    for (int b = 0; b < 4; ++b) r4<DIR>(x[b], x[4 + b], x[8 + b], x[12 + b]);
    r4<DIR>(x[0], x[1], x[2], x[3]);
    {
        c32 p = tws<DIR>(x[5], w1), q = tws<DIR>(x[7], w3), u = addr<DIR>(x[6], x[6]);
        c32 a0 = fma2(u, RR, x[4]), a1 = fma2(u, -RR, x[4]), a2 = p + q, a3 = p - q;
        x[4] = a0 + a2; x[6] = a0 - a2; x[5] = addr<DIR>(a1, a3); x[7] = subr<DIR>(a1, a3);
    }
    r4w8<DIR>(x[8], x[9], x[10], x[11]);
    {
        c32 p = tws<DIR>(x[13], w3), q = tws<DIR>(x[15], w9), u = nsubr<DIR>(x[14]);
        c32 a0 = fma2(u, RR, x[12]), a1 = fma2(u, -RR, x[12]), a2 = p + q, a3 = p - q;
        x[12] = a0 + a2; x[14] = a0 - a2; x[13] = addr<DIR>(a1, a3); x[15] = subr<DIR>(a1, a3);
    }
    c32 t;
    t = x[1]; x[1] = x[4]; x[4] = t;
    t = x[2]; x[2] = x[8]; x[8] = t;
    t = x[3]; x[3] = x[12]; x[12] = t;
    t = x[6]; x[6] = x[9]; x[9] = t;
    t = x[7]; x[7] = x[13]; x[13] = t;
    t = x[11]; x[11] = x[14]; x[14] = t;
}
template <int DIR> DEVI void r5(c32& x0, c32& x1, c32& x2, c32& x3, c32& x4) {
    constexpr float c1 = 0.30901699437494742410f, c2 = -0.80901699437494742410f;
    constexpr float s1 = 0.95105651629515357212f, s2 = 0.58778525229247312917f;
    const c32 C1 = {c1, c1}, C2 = {c2, c2}, S1 = {s1, s1}, S2 = {s2, s2};
    c32 t1 = x1 + x4, t2 = x2 + x3, t3 = x1 - x4, t4 = x2 - x3;
    c32 a1 = fma2(t2, C2, fma2(t1, C1, x0));
    c32 a2 = fma2(t2, C1, fma2(t1, C2, x0));
    c32 b1 = fma2(t4, S2, t3 * S1);
    c32 b2 = fma2(t4, -S1, t3 * S2);
    x0 = x0 + t1 + t2;
    x1 = addr<DIR>(a1, b1); x4 = subr<DIR>(a1, b1); x2 = addr<DIR>(a2, b2); x3 = subr<DIR>(a2, b2);
}
template <int DIR> DEVI void r10(c32 (&x)[10]) {
    c32 e0 = x[0] + x[5], o0 = x[0] - x[5];
    c32 e1 = x[2] + x[7], o1 = x[2] - x[7];
    c32 e2 = x[4] + x[9], o2 = x[4] - x[9];
    c32 e3 = x[6] + x[1], o3 = x[6] - x[1];
    c32 e4 = x[8] + x[3], o4 = x[8] - x[3];
    r5<DIR>(e0, e1, e2, e3, e4);
    r5<DIR>(o0, o1, o2, o3, o4);
    x[0] = e0; x[6] = e1; x[2] = e2; x[8] = e3; x[4] = e4;
    x[5] = o0; x[1] = o1; x[7] = o2; x[3] = o3; x[9] = o4;
}

template <int CTRL> DEVI float dpp_add(float v) {
    return v + __builtin_bit_cast(float, __builtin_amdgcn_update_dpp(0, __builtin_bit_cast(int, v), CTRL, 0xf, 0xf, true));
}
DEVI float row_sum16(float v) {
    v = dpp_add<0xB1>(v); v = dpp_add<0x4E>(v); v = dpp_add<0x141>(v); v = dpp_add<0x140>(v);
    return v;
}
DEVI float swap32_add(float a, float b) {
    auto r = __builtin_amdgcn_permlane32_swap(__builtin_bit_cast(unsigned, a), __builtin_bit_cast(unsigned, b), false, false);
    return __builtin_bit_cast(float, (unsigned)r[0]) + __builtin_bit_cast(float, (unsigned)r[1]);
}
DEVI float swap16_add(float a, float b) {
    auto r = __builtin_amdgcn_permlane16_swap(__builtin_bit_cast(unsigned, a), __builtin_bit_cast(unsigned, b), false, false);
    return __builtin_bit_cast(float, (unsigned)r[0]) + __builtin_bit_cast(float, (unsigned)r[1]);
}
DEVI float rdlane(float v, int l) { return __builtin_bit_cast(float, __builtin_amdgcn_readlane(__builtin_bit_cast(int, v), l)); }
DEVI float rdfirst(float v) { return __builtin_bit_cast(float, __builtin_amdgcn_readfirstlane(__builtin_bit_cast(int, v))); }
DEVI void wave_sum4(float& a, float& b, float& c, float& d) {
    float ab = swap32_add(a, b), cd = swap32_add(c, d);
    float t = row_sum16(swap16_add(ab, cd));
    a = rdlane(t, 0); c = rdlane(t, 16); b = rdlane(t, 32); d = rdlane(t, 48);
}
DEVI void wave_sum2(float& a, float& b) {
    float ab = swap32_add(a, b);
    float t = row_sum16(swap16_add(ab, ab));
    a = rdlane(t, 0); b = rdlane(t, 32);
}
#define POLL1 0
#define POLL2 0
#define T2E 0
#define T2C_DELAY 0
#define T1P_DELAY 0
#define NOCAN 1
#define T2_DELAY 4
#define GL_MOD 0
#define DSPLIT 0
#define ACT_T 0
#define RB_DIRECT 1
#define PC_TOP 1
#define PFN_POS 0
#define EARLY_WF 0
#define T1_DELAY 0
#define ACT_GAP 0
#define GEMV_TOP 1
#define FORCE_SLOW 0
#define AUX_FIRST 0
#define LN_TRIG 4u
#define LOW_POS 1
#define PRIO 1
#define FIN_SC1 0
#define STG_TRIG 4
#define DSTG_TRIG 6
#define PFG2_DELAY 0
#define RS_SLEEP 1
#define B7BAR 0
#define DSTG_CNT 4u
#define PFD_DELAY 0
#define DSTG_POS 2
#define RB_DELAY3 8
#define DN_LATE 0
#define DSTG_DELAY 0
#define RB_DELAY2 40
#define RB_STR 64
#define TLB_PF 0
#define UP 3
#define LOW_DELAY 0
#define X_DELAY 14
#define MOD_DELAY 2
#define CANARY_SLEEP 2
#define CANARY_DELAY 4
#define RB_DELAY 16
#define XS_DELAY 8
#define ACT_DELAY 24
#define STG_DELAY 0
#define MOD_DRAIN 0
#define QBITS 0
#define CODE_PF 12
#define PFG_DELAY 20
#define STG_CNT 4u
#define N_DELAY 0
#define PF_D 4
#define PF_G 1
#define PF_W 1
#define PF_N 1
#define W_POS 1
#define NON_POS 1
#define MODB_DELAY 40
#define STAMP_MASK 0xffffffffu
#define DBG_NOW 0
#define DBG_NON 0
#define DBG_NOS 0
#define DOWN_ROT 3
#define OUT_AUX 16

constexpr int WS_EPOCH = 0;
constexpr int WS_MODP = 4096;
constexpr int WS_MODG = 20480;
constexpr int WS_RB   = 65536;
constexpr int WS_XS   = 81920;
constexpr int XS_STR  = 2816;
constexpr int WS_ACT  = 104448;
constexpr int WS_SMALL_END = WS_ACT + 27648;
constexpr int WS_EG   = 4 << 20;
constexpr int WS_T1   = 1 << 20;
constexpr int WS_T2   = 8 << 20;
constexpr int REG_B   = 20608;
constexpr int WS_BYTES = 16 << 20;
constexpr unsigned SENT = 0xffffffffu;
constexpr int SPIN_CAP = 1 << 15;
constexpr int SC1 = 16;

constexpr int EXW = 672;
constexpr int L_EX  = 0;
constexpr int L_MA  = 21504;
constexpr int L_MB  = 26624;
constexpr int L_RED = 36864;
constexpr int L_STG = 37888;
constexpr int L_BIG = 58368;
constexpr int L_TW  = L_BIG + 69632;
constexpr int L_XW  = L_TW + 7168;
constexpr int L_TOTAL = L_BIG + 81920;

#define STAMP(i) do {} while (0)
#define DRAIN() asm volatile("s_waitcnt vmcnt(0)" ::: "memory")
#define PIN(x) asm volatile("" : "+v"(x))
#define DRAIN_C() __builtin_amdgcn_s_waitcnt(0x0f70)
#define SCHED() __builtin_amdgcn_sched_barrier(0)
#define CFENCE() asm volatile("" ::: "memory")
#define BAR() asm volatile("s_waitcnt lgkmcnt(0)\n\ts_barrier" ::: "memory")

DEVI void lds_inc(unsigned* p) { asm volatile("ds_add_u32 %0, %1" : : "v"((unsigned)(size_t)(LAS unsigned*)p), "v"(1u) : "memory"); }
DEVI unsigned lds_inc_rtn(unsigned* p) {
    unsigned r;
    asm volatile("ds_add_rtn_u32 %0, %1, %2\n\ts_waitcnt lgkmcnt(0)" : "=v"(r) : "v"((unsigned)(size_t)(LAS unsigned*)p), "v"(1u) : "memory");
    return r;
}
DEVI unsigned umax3(unsigned a, unsigned b, unsigned c) { return max(max(a, b), c); }
DEVI unsigned umed3(unsigned a, unsigned b, unsigned c) { return max(min(a, b), min(max(a, b), c)); }
DEVI unsigned rank16(unsigned k) {
    unsigned o = __builtin_amdgcn_update_dpp(k, k, 0x124, 2, 0xf, false);
    o = __builtin_amdgcn_update_dpp(o, k, 0x128, 4, 0xf, false);
    o = __builtin_amdgcn_update_dpp(o, k, 0x12c, 8, 0xf, false);
    unsigned n = (o > k) + ((unsigned)__builtin_amdgcn_update_dpp(0u, o, 0x121, 0xf, 0xf, true) > k)
               + ((unsigned)__builtin_amdgcn_update_dpp(0u, o, 0x122, 0xf, 0xf, true) > k) + ((unsigned)__builtin_amdgcn_update_dpp(0u, o, 0x123, 0xf, 0xf, true) > k);
    { auto r = __builtin_amdgcn_permlane32_swap(n, n, false, false); n = (unsigned)r[0] + (unsigned)r[1]; }
    { auto r = __builtin_amdgcn_permlane16_swap(n, n, false, false); n = (unsigned)r[0] + (unsigned)r[1]; }
    return n;
}
DEVI unsigned ord16x2(unsigned bits) {
    unsigned m, o;
    asm("v_pk_ashrrev_i16 %0, 15, %1 op_sel_hi:[0,1]" : "=v"(m) : "v"(bits));
    asm("v_pk_min_u16 %0, %1, %2" : "=v"(o) : "v"(bits ^ (m | 0x80008000u)), "s"(0xfffefffeu));
    return o;
}
DEVI void wave_sum6(float& a, float& b, float& c, float& d, float& e, float& f) {
    const float ab = swap32_add(a, b), cd = swap32_add(c, d), ef = swap32_add(e, f);
    float t = swap16_add(ab, cd), u = swap16_add(ef, ef);
    t = dpp_add<0xB1>(t); u = dpp_add<0xB1>(u); t = dpp_add<0x4E>(t); u = dpp_add<0x4E>(u);
    t = dpp_add<0x141>(t); u = dpp_add<0x141>(u); t = dpp_add<0x140>(t); u = dpp_add<0x140>(u);
    asm volatile("" : "+v"(t), "+v"(u));
    a = rdlane(t, 0); c = rdlane(t, 16); b = rdlane(t, 32); d = rdlane(t, 48); e = rdlane(u, 0); f = rdlane(u, 32);
}
DEVI u32x2 tagged(c32 v, unsigned tb) {
    return u32x2{(__float_as_uint(v.x) & 0xffffff00u) | tb, (__float_as_uint(v.y) & 0xffffff00u) | tb};
}
typedef _Float16 f16x2 __attribute__((ext_vector_type(2)));
DEVI unsigned packT(c32 v, unsigned tw) {
    return ((__builtin_bit_cast(unsigned, __builtin_convertvector(v, f16x2)) + 0x00020002u) & 0xfffcfffcu) | tw;
}
DEVI c32 unpackT(unsigned g, unsigned tw, unsigned& m) {
    const unsigned a = g ^ tw;
    m |= a;
    return __builtin_convertvector(__builtin_bit_cast(f16x2, a), c32);
}
constexpr unsigned TMASK = 0x00030003u;
DEVI c32 untag(u32x2 v, unsigned tb, unsigned& m) {
    const unsigned a = v.x ^ tb, b = v.y ^ tb;
    m |= a | b;
    return c32{__uint_as_float(a), __uint_as_float(b)};
}
#define GLM ""
DEVI void glds16(const void* gsrc, unsigned lds_dst) {
    unsigned keep;
    asm volatile("s_mov_b32 %0, m0\n\ts_mov_b32 m0, %2\n\ts_nop 0\n\tglobal_load_lds_dwordx4 %1, off" GLM "\n\ts_mov_b32 m0, %0" : "=&s"(keep) : "v"(gsrc), "s"(lds_dst) : "memory");
}
typedef u32x4 u32x4_u __attribute__((aligned(2)));
DEVI u32x4 ldu16(const u16* p) { return *(const u32x4_u*)p; }
DEVI c32 ldsr(const c32* p) { return *(const volatile LAS c32*)p; }
DEVI void sw32(c32& a, c32& b) {
    auto rx = __builtin_amdgcn_permlane32_swap(__float_as_uint(a.x), __float_as_uint(b.x), false, false);
    auto ry = __builtin_amdgcn_permlane32_swap(__float_as_uint(a.y), __float_as_uint(b.y), false, false);
    a = c32{__uint_as_float(rx[0]), __uint_as_float(ry[0])}; b = c32{__uint_as_float(rx[1]), __uint_as_float(ry[1])};
}
DEVI void sw16(c32& a, c32& b) {
    auto rx = __builtin_amdgcn_permlane16_swap(__float_as_uint(a.x), __float_as_uint(b.x), false, false);
    auto ry = __builtin_amdgcn_permlane16_swap(__float_as_uint(a.y), __float_as_uint(b.y), false, false);
    a = c32{__uint_as_float(rx[0]), __uint_as_float(ry[0])}; b = c32{__uint_as_float(rx[1]), __uint_as_float(ry[1])};
}
#define NOP10(x) asm volatile("s_nop 1" : "+v"(x[0]), "+v"(x[1]), "+v"(x[2]), "+v"(x[3]), "+v"(x[4]), "+v"(x[5]), "+v"(x[6]), "+v"(x[7]), "+v"(x[8]), "+v"(x[9]))
DEVI c32 isub(c32 a, c32 b) { c32 r; asm("v_pk_add_f32 %0, %1, %2 op_sel:[1,1] op_sel_hi:[0,0] neg_lo:[1,0] neg_hi:[0,1]" : "=v"(r) : "v"(a), "v"(b)); return r; }
DEVI c32 mul_lo(c32 a, c32 m) { c32 r; asm("v_pk_mul_f32 %0, %1, %2 op_sel:[0,0] op_sel_hi:[1,0]" : "=v"(r) : "v"(a), "v"(m)); return r; }
DEVI c32 mul_hi(c32 a, c32 m) { c32 r; asm("v_pk_mul_f32 %0, %1, %2 op_sel:[0,1] op_sel_hi:[1,1]" : "=v"(r) : "v"(a), "v"(m)); return r; }
DEVI c32 muls_lo(c32 p, c32 s) { c32 r; asm("v_pk_mul_f32 %0, %1, %2 op_sel:[0,0] op_sel_hi:[0,1]" : "=v"(r) : "v"(p), "s"(s)); return r; }
DEVI c32 muls_hi(c32 p, c32 s) { c32 r; asm("v_pk_mul_f32 %0, %1, %2 op_sel:[1,0] op_sel_hi:[1,1]" : "=v"(r) : "v"(p), "s"(s)); return r; }
DEVI c32 fmas_lo(c32 p, c32 s, c32 c) { c32 r; asm("v_pk_fma_f32 %0, %1, %2, %3 op_sel:[0,0,0] op_sel_hi:[0,1,1]" : "=v"(r) : "v"(p), "s"(s), "v"(c)); return r; }
DEVI c32 fmas_hi(c32 p, c32 s, c32 c) { c32 r; asm("v_pk_fma_f32 %0, %1, %2, %3 op_sel:[1,0,0] op_sel_hi:[1,1,1]" : "=v"(r) : "v"(p), "s"(s), "v"(c)); return r; }
DEVI c32 splat(float v) { return c32{v, v}; }
DEVI c32 unpk(unsigned v) { return c32{bf2f(v), bfhi(v)}; }

DEVI void cross4_fwd(c32 (&R)[10], bool odd) {
    NOP10(R);
    c32 S[5], D[5];
#pragma unroll
    for (int i = 0; i < 5; ++i) { sw32(R[i], R[i + 5]); S[i] = R[i] + R[i + 5]; D[i] = R[i] - R[i + 5]; }
    D[4] = odd ? c32{D[4].y, -D[4].x} : D[4];
    sw16(S[0], S[1]); R[0] = S[0] + S[1]; R[1] = S[0] - S[1];
    sw16(S[2], S[3]); R[2] = S[2] + S[3]; R[3] = S[2] - S[3];
    sw16(D[0], D[1]); R[4] = add_mi(D[0], D[1]); R[5] = add_pi(D[0], D[1]);
    sw16(D[2], D[3]); R[6] = add_mi(D[2], D[3]); R[7] = add_pi(D[2], D[3]);
    sw16(S[4], D[4]); R[8] = S[4] + D[4]; R[9] = S[4] - D[4];
}
DEVI void cross4_inv(c32 (&R)[10], bool odd) {
    c32 T[10];
    T[0] = R[0] + R[1]; T[1] = R[0] - R[1];
    T[2] = R[2] + R[3]; T[3] = R[2] - R[3];
    T[4] = R[4] + R[5]; T[5] = isub(R[4], R[5]);
    T[6] = R[6] + R[7]; T[7] = isub(R[6], R[7]);
    T[8] = R[8] + R[9]; T[9] = R[8] - R[9];
    NOP10(T);
#pragma unroll
    for (int i = 0; i < 5; ++i) sw16(T[2 * i], T[2 * i + 1]);
    T[9] = odd ? c32{-T[9].y, T[9].x} : T[9];
    R[0] = T[0] + T[4]; R[5] = T[0] - T[4];
    R[1] = T[1] + T[5]; R[6] = T[1] - T[5];
    R[2] = T[2] + T[6]; R[7] = T[2] - T[6];
    R[3] = T[3] + T[7]; R[8] = T[3] - T[7];
    R[4] = T[8] + T[9]; R[9] = T[8] - T[9];
#pragma unroll
    for (int i = 0; i < 5; ++i) sw32(R[i], R[i + 5]);
}

DEVI void row_stats(c32 s, c32 q, c32 piv, c32& mean, c32& den, c32& rinv) {
    const float inv = 1.0f / 640.0f;
    const c32 ms = s * inv;
    c32 var = q * inv - ms * ms;
    var = c32{fmaxf(var.x, 0.f), fmaxf(var.y, 0.f)};
    mean = rbf2(piv + ms);
    const c32 ve = rbf2(rbf2(var) + 1e-5f);
    den = rbf2(c32{__builtin_amdgcn_sqrtf(ve.x), __builtin_amdgcn_sqrtf(ve.y)});
    rinv = c32{__builtin_amdgcn_rcpf(den.x), __builtin_amdgcn_rcpf(den.y)};
}
DEVI c32 ln_norm(c32 x, float mean, float den, float rinv) {
    const c32 d = rbf2(x - mean);
    c32 qv = d * rinv;
    qv = fma2(fma2(-qv, splat(den), d), splat(rinv), qv);
    return rbf2(qv);
}
DEVI float silu_bf(unsigned cbits) {
    const float c = bf2f(cbits);
    return rbf(c * __builtin_amdgcn_rcpf(1.0f + __expf(-c)));
}
template <int NP, int LPR>
DEVI unsigned gemv_part(const u32x4 (&wq)[NP], const float (&sc)[NP], float* red, unsigned* cnt, int aw, int lane) {
    float acc[8];
#pragma unroll
    for (int j = 0; j < 8; ++j) acc[j] = 0.f;
#pragma unroll
    for (int i = 0; i < NP; ++i)
#pragma unroll
        for (int j = 0; j < 4; ++j) {
            acc[2 * j] = fmaf(sc[i], bf2f(wq[i][j]), acc[2 * j]);
            acc[2 * j + 1] = fmaf(sc[i], bfhi(wq[i][j]), acc[2 * j + 1]);
        }
    float a0 = swap32_add(acc[0], acc[4]), a1 = swap32_add(acc[1], acc[5]), a2 = swap32_add(acc[2], acc[6]), a3 = swap32_add(acc[3], acc[7]);
    float b0 = swap16_add(a0, a2), b1 = swap16_add(a1, a3);
    if (LPR <= 4) { b0 = dpp_add<0x124>(b0); b1 = dpp_add<0x124>(b1); }
    b0 = dpp_add<0x128>(b0); b1 = dpp_add<0x128>(b1);
    if (LPR == 2) { b0 = dpp_add<0x4E>(b0); b1 = dpp_add<0x4E>(b1); }
    if ((lane & 15) < LPR) {
        const int j = (lane >> 5) * 4 + ((lane >> 4) & 1) * 2;
        *(c32*)(red + aw * (8 * LPR) + (lane & 15) * 8 + j) = c32{b0, b1};
    }
    unsigned old = 0;
    CFENCE();
    if (lane == 0) old = lds_inc_rtn(cnt);
    old = __builtin_amdgcn_readfirstlane(old);
    CFENCE();
    return old;
}

__global__ __launch_bounds__(512, 2) void fused(
        const u16* wmod, const u16* cvec, const u16* x, unsigned char* ws,
        const u16* wr, const u16* wi, const u16* wrt, u16* out,
        const u16* wg, const u16* wu, const u16* wd) {
    extern __shared__ __attribute__((aligned(16))) unsigned char smem[];
    const int tid = threadIdx.x, lane = tid & 63, b = blockIdx.x;
    typedef const __attribute__((address_space(1))) unsigned* gptr;
    unsigned long long pc; unsigned len;
    asm volatile("s_getpc_b64 %0\n.Lpc_here:\n\ts_mov_b32 %1, .Lfused_end-.Lpc_here\n\ts_max_u32 %1, %1, .Lgu_end-.Lpc_here\n\ts_max_u32 %1, %1, .Ldn_end-.Lpc_here" : "=s"(pc), "=s"(len) : : "scc");
    unsigned pcf;
    pcf = *(gptr)(pc + min((unsigned)(((b >> 3) * CODE_PF + min(tid, CODE_PF - 1)) * 128), len));
    SCHED();
    const int wave = __builtin_amdgcn_readfirstlane(tid >> 6);
    const int ht = tid & 255;
    STAMP(0);
    const bool is_col = b < 161;
    const int mrole = b - 161;
    const int s0 = b;
    const bool spec = b < 8 || b == 160;
    float* red = (float*)(smem + L_RED);
    unsigned* cnts = (unsigned*)red + 192;
    const int r8 = b >> 3, x8 = b & 7;
    const int slot = x8 * 30 + r8 - 1 - (r8 > 20);
    const bool has_u = r8 != 0 && r8 != 20;
    const int cg0 = x8 ? 7 * x8 - 1 : 0, cg = cg0 + r8 - (x8 ? 20 : 21);
    const bool is_down = mrole >= 54 && mrole < 94;

    unsigned xa[5], xb[5]; c32 tA[15], tB[9]; c32 v[16]; c32 piv;
    u32x4 wl[4];
    const int nl = min(lane, 39), ka = lane & 15, g1 = lane >> 5;
    const bool g0 = (lane >> 4) & 1;
    c32 tw4; float tcv;
#define TCV(i) c32{rdlane(tcv, 2 * (i)), rdlane(tcv, 2 * (i) + 1)}

    unsigned tag, tb, tw;
#define GET_TAG(ev) do { PIN(ev); const unsigned epoch_ = __builtin_amdgcn_readfirstlane(ev); \
        tag = ((epoch_ * 0x9E3779B1u + 0x7F4A7C15u) & 0xffffff00u) | ((epoch_ + 1u) & 0xffu); tb = tag & 0xffu; tw = (tag & 3u) | (((tag + 1u) & 3u) << 16); PIN(tw); } while (0)
#define ROWSYNC(c, n) do { CFENCE(); \
        if (lane == 0) lds_inc(cnts + (c)); \
        while (__hip_atomic_load((LAS unsigned*)(cnts + (c)), __ATOMIC_RELAXED, __HIP_MEMORY_SCOPE_WORKGROUP) < (n)) __builtin_amdgcn_s_sleep(RS_SLEEP); \
        CFENCE(); } while (0)
#define LOAD_W(cond) do { if (cond) { \
            const int gi = b & 7, j8 = (ht >> 1) * 8; \
            const bool mir = gi >= 2 && (gi & 1); \
            const int res = gi < 2 ? gi * 4 : gi >> 1; \
            unsigned off = (unsigned)((mir ? (1024 - j8 - res) * 321 + 305 - c16 : (j8 + res) * 321 + c16) + (ht & 1) * 8); \
            const unsigned o0 = mir ? 1024 * 321 : 0, o1 = mir ? 0 : 1024 * 321; \
            wl[0] = ldu16(wr + o0 + off); wl[1] = ldu16(wr + o1 + off); \
            wl[2] = ldu16(wi + o0 + off); wl[3] = ldu16(wi + o1 + off); \
            } } while (0)
#define PREF_G() do {     \
            pg0 = *(const unsigned*)(((ht & 4) ? wu : wg) + (unsigned)((20 * r8 + min(ht >> 3, 19)) * 1728 + min((cg0 & ~1) * 32 + (ht & 3) * 64, 1726))); \
        } while (0)
    unsigned pf0, pn0, pg0;
    u32x4 wq[2], tbl[2]; unsigned cq, epv, epa, xl[10]; float tcl;
    auto pro_aux = [&]() {
        const __amdgpu_buffer_rsrc_t rm = __builtin_amdgcn_make_buffer_rsrc((void*)wmod, 0, 640 * 3840 * 2, 0x00020000);
        const __amdgpu_buffer_rsrc_t rc = __builtin_amdgcn_make_buffer_rsrc((void*)cvec, 0, 1280, 0x00020000);
        const int g = ht >> 1, ks = slot / 80;
        const int cofs = ((slot - ks * 80) * 16 + (ht & 1) * 8) * 2;
        const int row = (has_u && g < 107) ? ks * 214 + 2 * g : 640;
        cq = __builtin_amdgcn_raw_buffer_load_b32(rc, row * 2, 0, 0);
        wq[0] = __builtin_amdgcn_raw_buffer_load_b128(rm, row * 7680 + cofs, 0, 0);
        wq[1] = __builtin_amdgcn_raw_buffer_load_b128(rm, row * 7680 + cofs, 7680, 0);
        epa = *(const unsigned*)(ws + WS_EPOCH + (ht >> 8));
        if (ht < 8) cnts[ht] = 0u;
        if (PRIO) __builtin_amdgcn_s_setprio(3);
    };
    auto pro_row = [&]() {
        tbl[0] = ((const u32x4*)TWV)[ht];
        tbl[1] = ((const u32x4*)TWV)[min(256 + ht, 439)];
        tcl = TC[s0 * 32 + (lane & 31)];
        SCHED();
        const unsigned* xr = (const unsigned*)(x + (unsigned)((s0 + 256 * wave) * 640));
        if (X_DELAY) { asm volatile("" : : "s"(xr) : "memory"); __builtin_amdgcn_s_sleep(X_DELAY); CFENCE(); }
        if (PRIO) __builtin_amdgcn_s_setprio(2);
#pragma unroll
        for (int i = 0; i < 5; ++i) { xl[i] = xr[64 * i + lane]; xl[5 + i] = xr[1024 * 320 + 64 * i + lane]; }
    };
    if (wave >= 4) pro_aux(); else pro_row();
    BAR();
    unsigned char* ws2 = ws;
    asm volatile("" : "+s"(ws2) : "s"(wg), "s"(wu), "s"(wd));
    const __amdgpu_buffer_rsrc_t rs = __builtin_amdgcn_make_buffer_rsrc(ws2, 0, WS_BYTES, 0x00020000);
    if (wave >= 4) {
        CFENCE();
        {
            if (PC_TOP) PIN(pcf);
            PIN(cq);
            const float sc[2] = {silu_bf(cq), silu_bf(cq >> 16)};
            GET_TAG(epa); STAMP(22);
            int po = (has_u & (lane < 8)) ? WS_MODP + slot * 64 + lane * 8 : WS_BYTES;
            PIN(po);
            const bool pub = gemv_part<2, 2>(wq, sc, red, cnts, wave - 4, lane) == 3;
            const float* rp = red + 2 * (lane & 7);
            const c32 s = *(const c32*)rp + *(const c32*)(rp + 16) + *(const c32*)(rp + 32) + *(const c32*)(rp + 48);
            __builtin_amdgcn_raw_buffer_store_b64(tagged(s, tb), rs, pub ? po : WS_BYTES, 0, SC1);
        }
        STAMP(23);
        if (PRIO) __builtin_amdgcn_s_setprio(0);
        ((unsigned*)red)[152] = epa;
        CFENCE();
        if (lane == 0) lds_inc(cnts + 7);
    }
    asm volatile("s_nop 0");
    int r8q = r8; asm volatile("" : "+s"(r8q));
    const int cc = r8q < 20 ? 40 * (x8 >> 1) + 8 * (r8q >> 2) + 4 * (x8 & 1) + (r8q & 3) : 160;
    const int c16 = 16 * (r8q < 16 ? (r8q >> 3) * 10 + ((r8q >> 2) & 1) * 2 + (r8q & 1) + 5 * ((r8q >> 1) & 1) : 4 + 5 * ((r8q >> 1) & 1) + 10 * (r8q & 1));
    c32* const e = (c32*)(smem + L_EX) + (wave & 3) * EXW;
    const int baseA = (int)g0 + 5 * g1, baseB = 4 + 5 * g1 + 10 * (int)g0;
    constexpr int KC[10] = {0, 20, 2, 22, 10, 30, 12, 32, 0, 20};
    if (wave >= 4) {
        unsigned ptl = 0;
        STAMP(28);
        auto low_prio = [&]() {
            const int rho = x8 < 2 ? x8 * 4 : (x8 & 1) ? 8 - (x8 >> 1) : x8 >> 1;
            const int ra = ht >> 4, j8 = 8 * (8 * r8 + (ra & 7)), sel = ht & 15;
            const u16* src = (ra & 8) ? wi : wr;
            int o = (j8 + rho) * 321 + min(sel * 64, 320);
            if (sel == 11) o = ((2048 - j8 - rho) & 2047) * 321 + ((x8 >= 2 && (x8 & 1)) ? 320 : 0);
            if (sel == 12 && x8 == 0) o = (j8 + 4) * 321 + 320;
            pf0 = src[(unsigned)o];
            const int co = ht < 160 ? WS_EG + b * 5120 + ht * 16 : (ht < 186 && b < 160) ? WS_RB + (b * 26 + ht - 160) * 16 : WS_BYTES;
            __builtin_amdgcn_raw_buffer_store_b128(u32x4{SENT, SENT, SENT, SENT}, rs, co, 0, SC1);
        };
        if (!LOW_POS) low_prio();
        STAMP(1);
        while (__hip_atomic_load((LAS unsigned*)(cnts + 5), __ATOMIC_RELAXED, __HIP_MEMORY_SCOPE_WORKGROUP) < LN_TRIG) __builtin_amdgcn_s_sleep(1);
        __builtin_amdgcn_s_sleep(MOD_DELAY);
        STAMP(21);
        f32x4 m0, m1;
        const int ho = wave == 7 ? WS_MODP + lane * 16 : WS_BYTES;
        const unsigned th = wave == 7 ? tb : 0u;
        auto poll_mod = [&](bool first) {
            CFENCE();
            u32x4 g[UP], h[UP];
#pragma unroll
            for (int i = 0; i < UP; ++i) g[i] = __builtin_amdgcn_raw_buffer_load_b128(rs, WS_MODP + 1024 + ht * 16, i * 5120, SC1);
#pragma unroll
            for (int i = 0; i < UP; ++i) h[i] = __builtin_amdgcn_raw_buffer_load_b128(rs, ho, i * 5120, SC1);
            if (first) { CFENCE(); low_prio(); CFENCE(); }
            unsigned m = 0;
            m0 = f32x4{0.f, 0.f, 0.f, 0.f}; m1 = m0;
#pragma unroll
            for (int i = 0; i < UP; ++i) {
                const u32x4 a = g[i] ^ tb, c = h[i] ^ th;
                m |= a.x | a.y | a.z | a.w | c.x | c.y | c.z | c.w;
                m0 += __builtin_bit_cast(f32x4, a); m1 += __builtin_bit_cast(f32x4, c);
            }
            return (bool)__all((m & 0xffu) == 0u);
        };
        if (!(LOW_POS && poll_mod(true)))
            for (int spin = 0; spin < SPIN_CAP; ++spin) if (poll_mod(false)) break;
        {
            const bool isc = ht >= 96;
            const c32 p0 = rbf2(c32{m0.x, m0.y}), p1 = rbf2(c32{m0.z, m0.w});
            const c32 q0 = isc ? rbf2(p0 + 1.f) * 0.0625f : p0 * 0.25f, q1 = isc ? rbf2(p1 + 1.f) * 0.0625f : p1 * 0.25f;
            *(f32x4*)(smem + L_MA + 1024 + ht * 16) = f32x4{q0.x, q0.y, q1.x, q1.y};
            if (wave == 7) {
                const c32 r0 = rbf2(c32{m1.x, m1.y}) * 0.25f, r1 = rbf2(c32{m1.z, m1.w}) * 0.25f;
                *(f32x4*)(smem + L_MA + lane * 16) = f32x4{r0.x, r0.y, r1.x, r1.y};
            }
        }
        STAMP(2);
        BAR();
        LOAD_W(b < 160);
        if (spec) {
            const bool mir = b >= 2 && (b & 1);
            const int res = b < 2 ? b * 4 : b >> 1;
            const unsigned po = (unsigned)(b == 160 ? 4 * ht * 321 + 320 : mir ? (8 * ht + res) * 321 + 320 : ((2048 - 8 * ht - res) & 2047) * 321);
            unsigned* xw = (unsigned*)(smem + L_XW);
            const unsigned s0v = (unsigned)wr[po] | ((unsigned)wi[po] << 16);
            unsigned s1v = 0;
            if (b == 160) s1v = (unsigned)wr[po + 1024 * 321] | ((unsigned)wi[po + 1024 * 321] << 16);
            xw[ht] = s0v; xw[256 + ht] = s1v;
            if (ht == 0) xw[512] = s0v;
        }
        if (r8 > 20) {
            __builtin_amdgcn_s_sleep(PFD_DELAY);
            const int cb = 80 * ((x8 + 1) & 7) + (ht & 1) * 78, row = (r8 - 21) * 158 + (ht >> 1);
            unsigned pd0 = *(const unsigned*)(wd + (unsigned)(row * 640 + cb));
            unsigned pd1 = *(const unsigned*)(wd + (unsigned)(min((r8 - 21) * 158 + 128 + min(ht >> 1, 29), 1727) * 640 + cb));
            PIN(pd0); PIN(pd1);
        }
        PIN(pf0);
        if (b < 160) {
#pragma unroll
            for (int i = 0; i < 4; ++i) *(u32x4*)(smem + L_BIG + 16 * 272 * 8 + (i * 256 + ht) * 16) = wl[i];
        }
        DRAIN();
        if (EARLY_WF && is_col) ROWSYNC(3, 4u);
        else BAR();
    } else {
        PIN(pcf);
        *(u32x4*)(smem + L_TW + ht * 16) = tbl[0];
        *(u32x4*)(smem + L_TW + 4096 + ht * 16) = tbl[1];
        CFENCE();
        if (lane == 0) lds_inc(cnts + 7);
#pragma unroll
        for (int i = 0; i < 5; ++i) { xa[i] = xl[i]; xb[i] = xl[5 + i]; PIN(xa[i]); PIN(xb[i]); }
        piv = c32{rdfirst(bf2f(xa[0])), rdfirst(bf2f(xb[0]))};
        c32 s_a = {0.f, 0.f}, q_a = s_a, s_b = s_a, q_b = s_a;
#pragma unroll
        for (int i = 0; i < 5; ++i) {
            const c32 da = unpk(xa[i]) - piv.x, db = unpk(xb[i]) - piv.y;
            s_a += da; q_a = fma2(da, da, q_a); s_b += db; q_b = fma2(db, db, q_b);
        }
        float sa = s_a.x + s_a.y, sb = s_b.x + s_b.y, qa = q_a.x + q_a.y, qb = q_b.x + q_b.y;
        wave_sum4(sa, sb, qa, qb);
        c32 mean, den, rinv;
        row_stats(c32{sa, sb}, c32{qa, qb}, piv, mean, den, rinv);
        float* sd = (float*)(smem + L_STG);
#pragma unroll
        for (int i = 0; i < 5; ++i) {
            const c32 na = ln_norm(unpk(xa[i]), mean.x, den.x, rinv.x), nb = ln_norm(unpk(xb[i]), mean.y, den.y, rinv.y);
            *(c32*)(sd + wave * 640 + 128 * i + 2 * lane) = na + nb;
            *(c32*)(sd + (4 + wave) * 640 + 128 * i + 2 * lane) = na - nb;
        }
        STAMP(1);
        tcv = tcl; PIN(tcv);
        c32 cf[4];
#pragma unroll
        for (int q = 0; q < 3; ++q) cf[q] = TCV(wave * 4 + q);
        cf[3] = TCV(wave == 0 ? 1 : wave * 4 + 3);
        tw4 = TCV(3);
        CFENCE();
        if (lane == 0) lds_inc(cnts + 5);
        while (__hip_atomic_load((LAS unsigned*)(cnts + 7), __ATOMIC_RELAXED, __HIP_MEMORY_SCOPE_WORKGROUP) < 8u) __builtin_amdgcn_s_sleep(1);
        CFENCE();
        epv = ((const unsigned*)red)[152];
#pragma unroll
        for (int i = 0; i < 15; ++i) tA[i] = ((const c32*)(smem + L_TW))[i * 40 + nl];
#pragma unroll
        for (int i = 0; i < 9; ++i) tB[i] = ((const c32*)(smem + L_TW))[600 + i * 4 + (lane >> 4)];
        while (__hip_atomic_load((LAS unsigned*)(cnts + 5), __ATOMIC_RELAXED, __HIP_MEMORY_SCOPE_WORKGROUP) < 4u) __builtin_amdgcn_s_sleep(RS_SLEEP);
        CFENCE();
        const float* sp = sd + (wave & 1) * 2560 + nl;
        c32 p[32];
#pragma unroll
        for (int i = 0; i < 32; ++i) p[i] = c32{sp[(i & 3) * 640 + 80 * (i >> 2)], sp[(i & 3) * 640 + 80 * (i >> 2) + 40]};
        SCHED();
#pragma unroll
        for (int n1 = 0; n1 < 16; n1 += 2) {
            c32 a0 = muls_lo(p[2 * n1], cf[0]), a1 = muls_hi(p[2 * n1], cf[0]);
#pragma unroll
            for (int q = 1; q < 4; ++q) { a0 = fmas_lo(p[2 * n1 + q], cf[q], a0); a1 = fmas_hi(p[2 * n1 + q], cf[q], a1); }
            v[n1] = a0; v[n1 + 1] = a1;
        }
        GET_TAG(epv);
        STAMP(2);
        BAR();
        const float* ma = (const float*)(smem + L_MA) + nl;
        c32 m[8], sv[8];
#pragma unroll
        for (int i = 0; i < 8; ++i) m[i] = c32{ma[640 + 80 * i], ma[680 + 80 * i]};
#pragma unroll
        for (int i = 0; i < 8; ++i) sv[i] = c32{ma[80 * i], ma[40 + 80 * i]};
        SCHED();
#pragma unroll
        for (int i = 0; i < 8; ++i) { v[2 * i] = mul_lo(v[2 * i], m[i]); v[2 * i + 1] = mul_hi(v[2 * i + 1], m[i]); }
        const float w0 = wave == 0 ? 1.f : 0.f;
#pragma unroll
        for (int i = 0; i < 8; ++i) { v[2 * i].x = fmaf(sv[i].x, w0, v[2 * i].x); v[2 * i + 1].x = fmaf(sv[i].y, w0, v[2 * i + 1].x); }
        r16<-1>(v);
        if (lane < 40) {
            e[lane] = v[0];
#pragma unroll
            for (int k = 1; k < 16; ++k) e[k * 42 + lane] = cmul(v[k], tA[k - 1]);
        }
        __builtin_amdgcn_wave_barrier();
        c32 R[10];
#pragma unroll
        for (int j = 0; j < 10; ++j) R[j] = ldsr(e + ka * 42 + 4 * j + (lane >> 4));
        __builtin_amdgcn_wave_barrier();
        r10<-1>(R);
#pragma unroll
        for (int k = 1; k < 10; ++k) R[k] = cmul(R[k], tB[k - 1]);
        cross4_fwd(R, g0);
        if (wave == 0) {
#pragma unroll
            for (int i = 0; i < 5; ++i) e[ka + 16 * ((i < 4 ? baseA : baseB) + KC[2 * i])] = R[2 * i + 1];
            if (lane == 0) {
                e[320] = R[0];
                __builtin_amdgcn_raw_buffer_store_b128(u32x4{packT(c32{R[1].x + R[1].x, 0.f}, tw), packT(tw4 * (R[1].y + R[1].y), tw), tag, tag}, rs, WS_T1 + 80 * 32768 + s0 * 128, 0, SC1);
            }
            __builtin_amdgcn_wave_barrier();
#pragma unroll
            for (int i = 0; i < 5; ++i) R[2 * i + 1] = ldsr(e + 320 - ka - 16 * ((i < 4 ? baseA : baseB) + KC[2 * i]));
            SCHED();
#pragma unroll
            for (int i = 0; i < 5; ++i) {
                const c32 zp = R[2 * i + 1], z = R[2 * i];
                R[2 * i] = add_cj(z, zp);
                R[2 * i + 1] = cmul_s(sub_cj_mi(z, zp), tw4);
            }
        }
        STAMP(3);
        const int sb1 = WS_T1 + s0 * 128 + wave * 20 * 32768;
#pragma unroll
        for (int r = 0; r < 10; ++r)
            __builtin_amdgcn_raw_buffer_store_b32(packT(R[r], tw), rs, g1 * 32768 + (lane & 31) * 4, sb1 + r * 65536, SC1);
        STAMP(4);
        {
            const __amdgpu_buffer_rsrc_t rm = __builtin_amdgcn_make_buffer_rsrc((void*)wmod, 0, 640 * 3840 * 2, 0x00020000);
            const int r = ht / 5, q = ht - 5 * r;
            pn0 = __builtin_amdgcn_raw_buffer_load_b32(rm, ht < 160 ? (32 * r8 + r) * 7680 + 2560 + (q * 8 + ((x8 + 1) & 7)) * 128 : 640 * 7680, 0, 0);
        }
        if (!(EARLY_WF == 2 && is_col)) __builtin_amdgcn_s_waitcnt(0x0f71);
        if (EARLY_WF && is_col) __builtin_amdgcn_s_sleep(T1_DELAY);
        BAR();
    }
    STAMP(5);
    const int n0 = (((mrole - 54) & 7) * 5 + ((mrole - 54) >> 3)) * 16;
    auto stage_down = [&]() {
        __builtin_amdgcn_s_sleep(DSTG_DELAY);
        const unsigned lbase = (unsigned)(size_t)(LAS unsigned char*)smem + L_BIG + (wave - 4) * 1024;
#pragma unroll 2
        for (int i = 0; i < 14; ++i) {
            if (DSPLIT && i == DSPLIT) { while (__hip_atomic_load((LAS unsigned*)(cnts + 2), __ATOMIC_RELAXED, __HIP_MEMORY_SCOPE_WORKGROUP) < 4u) __builtin_amdgcn_s_sleep(1); }
            const int piece = i * 256 + ht, row = min(piece >> 1, 1727);
            glds16(wd + (unsigned)(row * 640 + n0 + (piece & 1) * 8), lbase + i * 4096);
        }
    };
    if (is_down) {
        const int ns = mrole - 54;
        __builtin_amdgcn_s_sleep(N_DELAY);
        const u16* src = wmod + (unsigned)(1280 + ns * 64 + (tid & 7) * 8 + (tid >> 3) * 38400);
        u32x4 wn[10];
#pragma unroll
        for (int i = 0; i < 10; ++i) wn[i] = *(const u32x4*)(src + i * 3840);
        const u32x4 c4 = ldu16(cvec + (tid >> 3) * 10);
        const unsigned cn[5] = {c4.x, c4.y, c4.z, c4.w, *(const unsigned*)(cvec + (tid >> 3) * 10 + 8)};
        float sc[10];
#pragma unroll
        for (int i = 0; i < 5; ++i) { sc[2 * i] = silu_bf(cn[i]); sc[2 * i + 1] = silu_bf(cn[i] >> 16); }
        float* rn = (float*)(smem + L_EX);
        if (gemv_part<10, 8>(wn, sc, rn, cnts + 1, wave, lane) == 7 && lane < 32) {
            c32 s = *(const c32*)(rn + 2 * lane);
#pragma unroll
            for (int q = 1; q < 8; ++q) s += *(const c32*)(rn + 64 * q + 2 * lane);
            __builtin_amdgcn_raw_buffer_store_b64(u32x2{pk_bf16(s), tag}, rs, WS_MODG + ns * 256 + lane * 8, 0, SC1);
        }
        STAMP(25);
        if (DSTG_POS == 1 && wave >= 4) {
            while (__hip_atomic_load((LAS unsigned*)(cnts + 1), __ATOMIC_RELAXED, __HIP_MEMORY_SCOPE_WORKGROUP) < 8u) __builtin_amdgcn_s_sleep(2);
            stage_down();
        }
    }
    auto fetch_modb = [&]() {
        u32x4 g[3];
        for (int spin = 0; spin < SPIN_CAP; ++spin) {
            CFENCE();
            bool ok = true;
#pragma unroll
            for (int i = 0; i < 3; ++i) g[i] = __builtin_amdgcn_raw_buffer_load_b128(rs, WS_MODG + min(i * 256 + ht, 639) * 16, 0, SC1);
#pragma unroll
            for (int i = 0; i < 3; ++i) ok = ok && g[i].y == tag && g[i].w == tag;
            if (__all(ok)) break;
            __builtin_amdgcn_s_sleep(4);
        }
#pragma unroll
        for (int i = 0; i < 3; ++i) {
            const int n = min(i * 256 + ht, 639);
            const float add = (n >= 320 && n < 480) ? 1.f : 0.f;
            const c32 f01 = rbf2(unpk(g[i].x) + add), f23 = rbf2(unpk(g[i].z) + add);
            *(f32x4*)(smem + L_MB + n * 16) = f32x4{f01.x, f01.y, f23.x, f23.y};
        }
        asm volatile("s_waitcnt lgkmcnt(0)" ::: "memory");
        if (lane == 0) lds_inc(cnts + 4);
    };
    c32 ga2[5], sb2[5], cb2[5], gb2[5], cw[5], va[5], vb[5]; float sum_cw = 0.f, sum_sw = 0.f;
    if (wave >= 4) {
        if (is_col) {
            c32 w[16];
            c32* cx = (c32*)(smem + L_BIG);
            c32* cy = cx + 16 * 272;
            const int ct = ht >> 4, cu = ht & 15;
            const bool live = (b < 160) || (cu < 2);
            const unsigned twl = live ? tw : tag, mskl = live ? TMASK : cu < 4 ? 0xffffffffu : 0u;
            c32 wf[16], tw256[15];
            const bool mir = (b & 7) >= 2 && (b & 1);
            const unsigned sgn = mir ? 0x80000000u : 0u;
            {
                const u16* wp = (const u16*)cy + ct * 16 + (mir ? 15 - cu : cu);
#pragma unroll
                for (int i = 0; i < 16; ++i) wf[i] = c32{bf2f(wp[i * 256]), __uint_as_float(((unsigned)wp[4096 + i * 256] << 16) ^ sgn)} * 0x1p-8f;
#pragma unroll
                for (int i = 0; i < 15; ++i) tw256[i] = ((const c32*)(smem + L_TW))[640 + i * 16 + ct];
            }
            const unsigned* xw = (const unsigned*)(smem + L_XW);
            if (spec) {
                if (b == 160) {
                    const unsigned* pt = xw + 2 * ct + (cu & 1);
                    const unsigned* pp = xw + 32 - 2 * ct - (cu & 1);
#pragma unroll
                    for (int i = 0; i < 16; ++i) wf[i] = add_cj(unpk(pt[32 * i]), unpk(pp[480 - 32 * i])) * 0x1p-9f;
                } else if (cu == 0) {
#pragma unroll
                    for (int i = 0; i < 16; ++i) wf[i] = (wf[i] + unpk(xw[ct + 16 * i] ^ sgn ^ 0x80000000u) * 0x1p-8f) * 0.5f;
                }
            }
            __builtin_amdgcn_s_waitcnt(0xc07f);
            if (EARLY_WF) {
#pragma unroll
                for (int i = 0; i < 16; ++i) PIN(wf[i]);
                BAR();
            }
            if (T1P_DELAY && !spec) __builtin_amdgcn_s_sleep(T1P_DELAY);
            for (int spin = 0; spin < SPIN_CAP; ++spin) {
                CFENCE();
                unsigned m = 0;
                unsigned g[16];
#pragma unroll
                for (int i = 0; i < 16; ++i) g[i] = __builtin_amdgcn_raw_buffer_load_b32(rs, WS_T1 + (cc >> 1) * 32768 + (cc & 1) * 64 + ct * 128 + cu * 4, i * 2048, SC1);
#pragma unroll
                for (int i = 0; i < 16; ++i) w[i] = unpackT(g[i], twl, m);
                if (__all((m & mskl) == 0u)) break;
            }
            STAMP(6);
            if (PRIO) __builtin_amdgcn_s_setprio(3);
            r16<-1>(w);
            cx[ht] = w[0];
#pragma unroll
            for (int c = 1; c < 16; ++c) cx[c * 272 + ht] = cmul(w[c], tw256[c - 1]);
            BAR();
#pragma unroll
            for (int j = 0; j < 16; ++j) w[j] = ldsr(cx + ct * 272 + j * 16 + cu);
            r16<-1>(w);
#pragma unroll
            for (int i = 0; i < 16; ++i) w[i] = cmul(w[i], wf[i]);
            r16<1>(w);
            cy[ht] = w[0];
#pragma unroll
            for (int c = 1; c < 16; ++c) cy[c * 272 + ht] = cmulc(w[c], tw256[c - 1]);
            BAR();
#pragma unroll
            for (int j = 0; j < 16; ++j) w[j] = ldsr(cy + ct * 272 + j * 16 + cu);
            r16<1>(w);
            STAMP(7);
            {
                const int base = mskl ? WS_T2 + (ct >> 1) * REG_B + cc * 128 + (ct & 1) * 64 + cu * 4 : WS_BYTES;
#pragma unroll
                for (int i = 0; i < 16; ++i)
                    __builtin_amdgcn_raw_buffer_store_b32((packT(w[i], 0u) & ~mskl) | twl, rs, base, i * 8 * REG_B, SC1);
            }
            if (PRIO) __builtin_amdgcn_s_setprio(0);
            if (T2E) __builtin_amdgcn_s_sleep(T2E - 1); else DRAIN();
            BAR();
        } else {
            if (PF_G == 1 || (is_down && DSTG_POS == 2)) { while (__hip_atomic_load((LAS unsigned*)(cnts + (is_down ? DSTG_TRIG : STG_TRIG)), __ATOMIC_RELAXED, __HIP_MEMORY_SCOPE_WORKGROUP) < (is_down ? DSTG_CNT : STG_CNT)) __builtin_amdgcn_s_sleep(2); }
            if (!is_down) __builtin_amdgcn_s_sleep(STG_DELAY);
            const unsigned lbase = (unsigned)(size_t)(LAS unsigned char*)smem + L_BIG + (wave - 4) * 1024;
            if (mrole < 54) {
#pragma unroll 4
                for (int i = 0; i < 20; ++i) {
                    const int piece = i * 256 + ht, row = piece >> 3, pp = piece & 7;
                    const u16* src = (pp & 4) ? wu : wg;
                    glds16(src + (unsigned)(row * 1728 + cg * 32 + (pp & 3) * 8), lbase + i * 4096);
                }
            } else if (DSTG_POS == 2 && is_down) stage_down();
            STAMP(9);
        }
    } else {
        unsigned wrv[5];
#pragma unroll
        for (int i = 0; i < 5; ++i) wrv[i] = ((const unsigned*)wrt)[64 * i + lane];
        ((u32x2*)(smem + L_MA))[ht] = u32x2{0u, 0u};
        PIN(pn0);
        if (is_col) BAR();
        else __builtin_amdgcn_s_sleep(PFG_DELAY);
        __builtin_amdgcn_raw_buffer_store_b128(u32x4{SENT, SENT, SENT, SENT}, rs, ht < 160 ? WS_EG + b * 5120 + 2560 + ht * 16 : WS_BYTES, 0, SC1);
        if (PF_G == 1) PREF_G();
        if (is_col) BAR();
        else __builtin_amdgcn_s_sleep(MODB_DELAY);
        fetch_modb();
        PIN(pg0);
        while (__hip_atomic_load((LAS unsigned*)(cnts + 4), __ATOMIC_RELAXED, __HIP_MEMORY_SCOPE_WORKGROUP) < 4u) __builtin_amdgcn_s_sleep(1);
        CFENCE();
        const c32* mb = (const c32*)(smem + L_MB) + lane;
        c32 scw = {0.f, 0.f}, ssw = scw;
#pragma unroll
        for (int i = 0; i < 5; ++i) {
            ga2[i] = mb[64 * i]; sb2[i] = mb[320 + 64 * i]; cb2[i] = mb[640 + 64 * i]; gb2[i] = mb[960 + 64 * i];
            const c32 wv = unpk(wrv[i]);
            cw[i] = cb2[i] * wv; scw += cw[i]; ssw = fma2(sb2[i], wv, ssw);
            va[i] = unpk(xa[i]) - piv.x; vb[i] = unpk(xb[i]) - piv.y;
        }
        sum_cw = scw.x + scw.y; sum_sw = ssw.x + ssw.y;
        wave_sum2(sum_cw, sum_sw);
        STAMP(24);
        if (is_col) BAR();
    }
    STAMP(8);
    if (wave < 4) {
        const int sb2o = WS_T2 + (s0 >> 1) * REG_B + (s0 & 1) * 64 + wave * 40 * 128, vo2 = (lane >> 4) * 128 + ka * 4;
        if (NOCAN) { if (!is_col) __builtin_amdgcn_s_sleep(T2_DELAY); else if (T2C_DELAY) __builtin_amdgcn_s_sleep(T2C_DELAY); }
        else if (!is_col) {
            for (int spin = 0; spin < SPIN_CAP; ++spin) {
                CFENCE();
                unsigned m = 0;
                (void)unpackT(__builtin_amdgcn_raw_buffer_load_b32(rs, vo2, sb2o, SC1), tw, m);
                if (__all((m & TMASK) == 0u)) break;
                __builtin_amdgcn_s_sleep(CANARY_SLEEP);
            }
            __builtin_amdgcn_s_sleep(CANARY_DELAY);
        }
        c32 R[10], sp0, sp4;
        for (int spin = 0; spin < SPIN_CAP; ++spin) {
            CFENCE();
            unsigned m = 0;
            unsigned g[10]; u32x4 gs = {tw, tw, tag, tag};
#pragma unroll
            for (int r = 0; r < 10; ++r) g[r] = __builtin_amdgcn_raw_buffer_load_b32(rs, vo2 + (r & 7) * 512, sb2o + (r >> 3) * 4096, SC1);
            if (wave == 0) gs = __builtin_amdgcn_raw_buffer_load_b128(rs, 0, sb2o + 160 * 128, SC1);
#pragma unroll
            for (int r = 0; r < 10; ++r) R[r] = unpackT(g[r], tw, m);
            sp0 = unpackT(gs.x, tw, m); sp4 = unpackT(gs.y, tw, m);
            if (__all((m & TMASK) == 0u && gs.z == tag && gs.w == tag)) break;
        }
        if (lane == 0) lds_inc(cnts + 6);
        STAMP(9);
        if (wave == 0) {
#pragma unroll
            for (int i = 0; i < 5; ++i) {
                const c32 f0 = R[2 * i], gq = cmulc_s(R[2 * i + 1], tw4);
                e[320 - ka - 16 * ((i < 4 ? baseA : baseB) + KC[2 * i])] = cj_add_i(f0, gq);
                R[2 * i] = add_pi(f0, gq);
            }
            if (lane == 0) e[0] = add_pi(sp0, cmulc_s(sp4, tw4));
            __builtin_amdgcn_wave_barrier();
#pragma unroll
            for (int i = 0; i < 5; ++i) R[2 * i + 1] = ldsr(e + ka + 16 * ((i < 4 ? baseA : baseB) + KC[2 * i]));
            __builtin_amdgcn_wave_barrier();
        }
        cross4_inv(R, g0);
#pragma unroll
        for (int k = 1; k < 10; ++k) R[k] = cmulc(R[k], tB[k - 1]);
        r10<1>(R);
#pragma unroll
        for (int j = 0; j < 10; ++j) e[ka * 41 + 4 * j + (lane >> 4)] = R[j];
        __builtin_amdgcn_wave_barrier();
#pragma unroll
        for (int k = 0; k < 16; ++k) v[k] = ldsr(e + k * 41 + nl);
        SCHED();
#pragma unroll
        for (int k = 1; k < 16; ++k) v[k] = cmulc(v[k], tA[k - 1]);
        __builtin_amdgcn_wave_barrier();
        r16<1>(v);
        if (lane < 40) {
            float* pl = (float*)e + lane;
#pragma unroll
            for (int n1 = 0; n1 < 16; ++n1) { pl[40 * n1] = v[n1].x; pl[640 + 40 * n1] = v[n1].y; }
        }
        if (!B7BAR) ROWSYNC(5, 8u);
    }
    if (B7BAR) BAR();
    STAMP(10);
    unsigned* keyl = (unsigned*)(smem + L_RED) + 64;
    unsigned* pairl = (unsigned*)(smem + L_RED) + 72;
    unsigned* cntl = (unsigned*)(smem + L_RED) + 80;
    float* rvl = (float*)(smem + L_RED) + 128;
    unsigned oa[5], ob[5], keya = 0, keyb = 0; int swp = 0;
    unsigned kk[8];
    u32x4 bfr[20], af[5];
    const int g4 = lane >> 4, r4i = (lane & 15) >> 2, p4 = lane & 3;
    int aoff = 0; bool nice = false; unsigned prow = 0;
    const bool is_gu = mrole >= 0 && mrole < 54;
    float* part = (float*)(smem + L_EX);
    auto gu_mfma = [&]() {
        unsigned pw = (unsigned)(((wave - 4) * 36 + prow) * 80 + g4 * 16);
        PIN(pw);
#pragma unroll 1
        for (int spin = 0; spin < SPIN_CAP; ++spin) {
            unsigned m = 0;
#pragma unroll
            for (int i = 0; i < 5; ++i) m = umax3(umax3(m, af[i].x, af[i].y), af[i].z, af[i].w);
            if (__all(m != SENT)) break;
            CFENCE();
#pragma unroll
            for (int i = 0; i < 5; ++i) af[i] = __builtin_amdgcn_raw_buffer_load_b128(rs, aoff + i * 64, 0, SC1);
        }
        STAMP(16);
        f32x4 acc[4];
#pragma unroll
        for (int c4 = 0; c4 < 4; ++c4) acc[c4] = f32x4{0.f, 0.f, 0.f, 0.f};
#pragma unroll
        for (int i = 0; i < 5; ++i) {
#pragma unroll
            for (int c4 = 0; c4 < 4; ++c4)
                acc[c4] = __builtin_amdgcn_mfma_f32_16x16x32_bf16(__builtin_bit_cast(bf16x8_t, bfr[i * 4 + c4]), __builtin_bit_cast(bf16x8_t, af[i]), acc[c4], 0, 0, 0);
            __builtin_amdgcn_sched_barrier(0);
        }
#pragma unroll
        for (int c4 = 0; c4 < 4; ++c4) *(f32x4*)(smem + L_EX + pw + c4 * 720) = acc[c4];
    };
    auto gu_epi = [&](bool check) {
        const int j = 4 * (wave >> 1) + g4, f = 16 * (wave & 1) + ka;
        unsigned pr = (unsigned)(((wave & 1) * 9 + j) * 80 + ka * 4);
        int ao = (f & 1) ? WS_BYTES : WS_ACT + (cg * 4 + (f >> 3)) * 128 + j * 16 + (f & 7) * 2;
        PIN(pr); PIN(ao);
        BAR();
        c32 t[4];
#pragma unroll
        for (int q = 0; q < 4; ++q) t[q] = c32{*(const float*)(smem + L_EX + pr + q * 2880), *(const float*)(smem + L_EX + pr + q * 2880 + 1440)};
        if (check) {
            const u32x4 c4 = *(const u32x4*)((const unsigned*)red + 136);
            const bool ok = c4.x + c4.y + c4.z + c4.w == 8u;
            ao = ok ? ao : WS_BYTES;
            nice = __builtin_amdgcn_readfirstlane(ok);
        }
        const c32 gu = rbf2((t[0] + t[1]) + (t[2] + t[3]));
        const float sg = rbf(gu.x * __builtin_amdgcn_rcpf(1.0f + __expf(-gu.x)));
        const float a = sg * gu.y;
        const float an = __builtin_bit_cast(float, __builtin_amdgcn_update_dpp(0, __builtin_bit_cast(int, a), 0xB1, 0xf, 0xf, true));
        __builtin_amdgcn_raw_buffer_store_b32(pk_bf16(c32{a, an}), rs, ao, 0, SC1);
        STAMP(17);
    };
    if (wave < 4) {
        const c32 c0 = TCV(wave & 1), c1 = TCV(4 + wave), c2 = TCV(8 + wave), c3 = TCV(12 + wave);
        const c32* pl = (const c32*)(smem + L_EX) + lane;
        c32 ya[5], yb[5];
        const float SC = 2.0f * 4096.0f / (2048.0f * 640.0f);
        {
            c32 s_a = {0.f, 0.f}, q_a = s_a, t_a = s_a, s_b = s_a, q_b = s_a, t_b = s_a;
            int rbo = lane == 0 ? WS_RB + b * RB_STR + wave * 4 : WS_BYTES;
            PIN(rbo);
            c32 p[40];
#pragma unroll
            for (int i = 0; i < 40; ++i) p[i] = ldsr(pl + ((i >> 1) & 3) * EXW + (i & 1) * 320 + 64 * (i >> 3));
            SCHED();
#pragma unroll
            for (int i = 0; i < 5; ++i) {
                c32 E = p[8 * i] * c0.x, O = p[8 * i + 2] * c1.x;
                E = fma2(p[8 * i + 1], splat(c0.y), E); O = fma2(p[8 * i + 3], splat(c1.y), O);
                E = fma2(p[8 * i + 4], splat(c2.x), E); O = fma2(p[8 * i + 6], splat(c3.x), O);
                E = fma2(p[8 * i + 5], splat(c2.y), E); O = fma2(p[8 * i + 7], splat(c3.y), O);
                ya[i] = E + O; yb[i] = E - O;
                const c32 gs = ga2[i] * SC;
                const c32 da = fma2(ya[i], gs, va[i]), db = fma2(yb[i], gs, vb[i]);
                s_a += da; q_a = fma2(da, da, q_a); t_a = fma2(da, cw[i], t_a);
                s_b += db; q_b = fma2(db, db, q_b); t_b = fma2(db, cw[i], t_b);
            }
            STAMP(11);
            float sa = s_a.x + s_a.y, sb = s_b.x + s_b.y, qa = q_a.x + q_a.y, qb = q_b.x + q_b.y, ta = t_a.x + t_a.y, tb2 = t_b.x + t_b.y;
            wave_sum6(sa, sb, qa, qb, ta, tb2);
            c32 mean, den, rinv;
            row_stats(c32{sa, sb}, c32{qa, qb}, piv, mean, den, rinv);
            const c32 rr = (c32{ta, tb2} - (mean - piv) * sum_cw) * rinv + sum_sw;
            unsigned cp = ord16x2(pk_bf16(rr));
            __builtin_amdgcn_raw_buffer_store_b32(cp, rs, rbo, 0, SC1);
            SCHED();
            const unsigned ca = cp & 0xffffu, cb = cp >> 16;
            keya = (ca << 16) | (unsigned)(0xffff - (s0 + 256 * wave));
            keyb = (cb << 16) | (unsigned)(0xffff - (s0 + 256 * wave + 1024));
            if (lane == 0) {
                keyl[wave] = keya; keyl[wave + 4] = keyb;
                const c32 rb = rbf2(rr);
                rvl[wave] = rb.x; rvl[wave + 4] = rb.y;
                CFENCE();
                lds_inc(cnts + 2);
            }
        }
        STAMP(12);
        if (ACT_T && wave == 0) ((unsigned*)red)[153] = (unsigned)__builtin_amdgcn_s_memrealtime();
        swp = __builtin_amdgcn_readfirstlane(keyb > keya);
        if (swp) {
#pragma unroll
            for (int i = 0; i < 5; ++i) { const unsigned t = xa[i]; xa[i] = xb[i]; xb[i] = t; const c32 u = ya[i]; ya[i] = yb[i]; yb[i] = u; }
            piv = c32{piv.y, piv.x};
        }
        unsigned kl;
        auto do_row = [&](const unsigned (&xr)[5], const c32 (&yr)[5], float pv, unsigned (&o)[5], int half, bool first) {
            c32 x1[5], sm = {0.f, 0.f}, sq = sm;
#pragma unroll
            for (int i = 0; i < 5; ++i) {
                x1[i] = rbf2(unpk(xr[i]) + rbf2(rbf2(yr[i] * SC) * ga2[i]));
                const c32 d = x1[i] - pv;
                sm += d; sq = fma2(d, d, sq);
            }
            float s1 = sm.x + sm.y, s2 = sq.x + sq.y;
            wave_sum2(s1, s2);
            const float ms = s1 * (1.0f / 640.0f), mean = rbf(pv + ms);
            const float den = rbf(__builtin_amdgcn_sqrtf(rbf(rbf(fmaxf(s2 * (1.0f / 640.0f) - ms * ms, 0.f)) + 1e-5f))), rinv = __builtin_amdgcn_rcpf(den);
            if (first) {
                while (__hip_atomic_load((LAS unsigned*)(cnts + 2), __ATOMIC_RELAXED, __HIP_MEMORY_SCOPE_WORKGROUP) < 4u) __builtin_amdgcn_s_sleep(1);
                CFENCE();
                kl = keyl[lane & 7];
            }
            const int lr = __popcll(__ballot(kl > (first ? max(keya, keyb) : min(keya, keyb))) & 0xffull);
            const int ego = (lr < 2 ? WS_EG + b * 5120 + lr * 2560 : WS_BYTES) + lane * 4;
            unsigned h[5];
#pragma unroll
            for (int i = 0; i < 5; ++i) {
                h[i] = pk_bf16(rbf2(ln_norm(x1[i], mean, den, rinv) * cb2[i]) + sb2[i]);
                __builtin_amdgcn_raw_buffer_store_b32(h[i], rs, ego, 256 * i, SC1);
            }
            if (first) { __builtin_amdgcn_sched_barrier(0); STAMP(29); }
            unsigned* stg = (unsigned*)(smem + L_STG) + half * 1280 + wave * 320 + lane;
#pragma unroll
            for (int i = 0; i < 5; ++i) {
                const unsigned xp = pk_bf16(x1[i]);
                __builtin_amdgcn_raw_buffer_store_b32(xp, rs, ego, 1280 + 256 * i, SC1);
                o[i] = pk_bf16(x1[i] + rbf2(unpk(h[i]) * gb2[i]));
                stg[64 * i] = h[i]; stg[2560 + 64 * i] = xp;
            }
        };
        do_row(xa, ya, piv.x, oa, swp, true);
        do_row(xb, yb, piv.y, ob, 1 - swp, false);
        STAMP(13);
    } else {
        if (DSTG_POS == 3 && is_down) stage_down();
        if (!is_col) {
            DRAIN();
            if (lane == 0) lds_inc(cnts + 3);
            while (__hip_atomic_load((LAS unsigned*)(cnts + 3), __ATOMIC_RELAXED, __HIP_MEMORY_SCOPE_WORKGROUP) < 4u) __builtin_amdgcn_s_sleep(1);
            CFENCE();
            if (is_down) {
#pragma unroll
                for (int i = 0; i < 14; ++i) {
                    const int row = 32 * min(wave - 4 + 4 * i, 53) + 8 * g4 + r4i;
                    LAS unsigned char* p = (LAS unsigned char*)(smem + L_BIG) + row * 32 + p4 * 8;
                    const u32x2 lo = __builtin_bit_cast(u32x2, __builtin_amdgcn_ds_read_tr16_b64_v4i16((LAS s16x4*)p));
                    const u32x2 hi = __builtin_bit_cast(u32x2, __builtin_amdgcn_ds_read_tr16_b64_v4i16((LAS s16x4*)(p + 4 * 32)));
                    bfr[i] = u32x4{lo.x, lo.y, hi.x, hi.y};
                }
            } else {
#pragma unroll
                for (int i = 0; i < 5; ++i)
#pragma unroll
                    for (int c4 = 0; c4 < 4; ++c4) {
                        const int row = 32 * (5 * (wave - 4) + i) + 8 * g4 + r4i;
                        LAS unsigned char* p = (LAS unsigned char*)(smem + L_BIG) + row * 128 + (16 * c4 + 4 * p4) * 2;
                        const u32x2 lo = __builtin_bit_cast(u32x2, __builtin_amdgcn_ds_read_tr16_b64_v4i16((LAS s16x4*)p));
                        const u32x2 hi = __builtin_bit_cast(u32x2, __builtin_amdgcn_ds_read_tr16_b64_v4i16((LAS s16x4*)(p + 4 * 128)));
                        bfr[i * 4 + c4] = u32x4{lo.x, lo.y, hi.x, hi.y};
                    }
            }
            STAMP(11);
        }
        while (__hip_atomic_load((LAS unsigned*)(cnts + 2), __ATOMIC_RELAXED, __HIP_MEMORY_SCOPE_WORKGROUP) < 4u) __builtin_amdgcn_s_sleep(1);
        if (is_down) {
            __builtin_amdgcn_s_sleep(RB_DELAY3);
        } else if (is_gu) __builtin_amdgcn_s_sleep(RB_DELAY);
        else __builtin_amdgcn_s_sleep(RB_DELAY2);
        u32x4 g;
        for (int spin = 0; spin < SPIN_CAP; ++spin) {
            CFENCE();
            g = __builtin_amdgcn_raw_buffer_load_b128(rs, WS_RB + ht * RB_STR, 0, SC1);
            if (__all(umax3(max(g.x, g.y), g.z, g.w) != SENT)) break;
        }
#pragma unroll
        for (int p = 0; p < 4; ++p) {
            kk[p] = (g[p] << 16) | (unsigned)(0xffff - (ht + 256 * p));
            kk[p + 4] = (g[p] & 0xffff0000u) | (unsigned)(0xffff - (ht + 256 * p + 1024));
        }
        STAMP(26);
        if (PRIO) __builtin_amdgcn_s_setprio(3);
        if (is_gu || is_down) {
            u32x2* bk = (u32x2*)(smem + L_MA);
            {
                const unsigned h1 = umax3(kk[0], kk[1], kk[2]), h2 = umax3(kk[3], kk[4], kk[5]), h3 = max(kk[6], kk[7]);
                const unsigned l3 = umax3(umed3(kk[0], kk[1], kk[2]), umed3(kk[3], kk[4], kk[5]), min(kk[6], kk[7]));
                bk[ht] = u32x2{umax3(h1, h2, h3), max(umed3(h1, h2, h3), l3)};
            }
            u32x2 ent[4];
            for (;;) {
#pragma unroll
                for (int q = 0; q < 4; ++q) ent[q] = *(volatile LAS u32x2*)(bk + q * 64 + lane);
                if (__all(min(min(ent[0].x, ent[1].x), min(ent[2].x, ent[3].x)) != 0u)) break;
            }
            unsigned gk = max(umax3(ent[0].x, ent[1].x, ent[2].x), ent[3].x);
            { auto r = __builtin_amdgcn_permlane32_swap(gk, gk, false, false); gk = max((unsigned)r[0], (unsigned)r[1]); }
            { auto r = __builtin_amdgcn_permlane16_swap(gk, gk, false, false); gk = max((unsigned)r[0], (unsigned)r[1]); }
            const unsigned thr = __builtin_amdgcn_readlane(gk, __builtin_ctzll(__ballot(rank16(gk) == 7u) | (1ull << 63)));
            unsigned* lst = (unsigned*)(smem + L_EX + 12288) + (wave - 4) * 512;
            int base = 0;
#define APPEND(k) do { const unsigned long long mk = __ballot((k) >= thr); \
                const int pos = __builtin_amdgcn_mbcnt_hi((unsigned)(mk >> 32), __builtin_amdgcn_mbcnt_lo((unsigned)mk, 0)); \
                if ((k) >= thr) (lst + base)[pos] = (k); \
                base += __popcll(mk); } while (0)
#pragma unroll
            for (int q = 0; q < 4; ++q) APPEND(ent[q].x);
            const int base1 = base;
            if (__any(max(umax3(ent[0].y, ent[1].y, ent[2].y), ent[3].y) >= thr)) {
#pragma unroll
                for (int q = 0; q < 4; ++q) APPEND(ent[q].y);
            }
            __builtin_amdgcn_wave_barrier();
            const int ci = min(ka, base - 1);
            unsigned cand = *(volatile LAS unsigned*)(lst + ci);
            const int slo = WS_EG + (int)(~cand & 255u) * 5120 + (ci >= base1 ? 2560 : 0);
            if (is_gu) {
                aoff = slo + (lane >> 4) * 16 + (wave - 4) * 320;
#pragma unroll
                for (int i = 0; i < 5; ++i) af[i] = __builtin_amdgcn_raw_buffer_load_b128(rs, aoff + i * 64, 0, SC1);
            }
            cand = ka < base ? cand : 0u;
            const unsigned rk = rank16(cand);
            const unsigned t8 = __builtin_amdgcn_readlane(cand, __builtin_ctzll(__ballot(rk == 7u) | (1ull << 63)));
            if (wave == 4 && rk < 8u) { ((unsigned*)red)[144 + rk] = cand; ((int*)red)[160 + rk] = slo; }
            prow = min(rk, 8u);
            int c = base <= 16 - FORCE_SLOW ? 0 : 8;
#pragma unroll
            for (int j = 0; j < 8; ++j) c += __popcll(__ballot(kk[j] >= t8));
            if (lane == 0) ((unsigned*)red)[136 + wave - 4] = c;
            if (is_gu) gu_mfma();
        }
        STAMP(14);
    }
    if (is_gu && wave < 4) gu_epi(true);
    else if (is_gu || is_down) {
        BAR();
        const u32x4 c4 = *(const u32x4*)((const unsigned*)red + 136);
        nice = __builtin_amdgcn_readfirstlane(c4.x + c4.y + c4.z + c4.w) == 8u;
    }
    unsigned selmask = 0;
    if (nice) {
        const unsigned t8 = ((const unsigned*)red)[151];
        selmask = __builtin_amdgcn_readfirstlane(((keya >= t8) | ((keyb >= t8) << 4)) << (wave & 3));
    } else {
        unsigned lim = 0xffffffffu;
#pragma unroll 1
        for (int it = 0; it < 8; ++it) {
            if (wave >= 4) {
                unsigned cur = 0;
#pragma unroll
                for (int m = 0; m < 8; ++m) { const unsigned o = __builtin_amdgcn_readfirstlane(keyl[m]); if (o < lim && o > cur) cur = o; }
                int c = 0;
#pragma unroll
                for (int j = 0; j < 8; ++j) c += __popcll(__ballot(kk[j] > cur));
                if (lane == 0) cntl[it * 4 + wave - 4] = c;
            }
            BAR();
            const u32x4 cc4 = *(const u32x4*)(cntl + it * 4);
            const int rank = __builtin_amdgcn_readfirstlane(cc4.x + cc4.y + cc4.z + cc4.w);
            if (rank >= 8) break;
            unsigned cur = 0; int mi = 0;
#pragma unroll
            for (int m = 0; m < 8; ++m) { const unsigned o = __builtin_amdgcn_readfirstlane(keyl[m]); if (o < lim && o > cur) { cur = o; mi = m; } }
            selmask |= 1u << mi;
            if (tid < 160) {
                const int arr = tid >= 80, pc = tid - 80 * arr;
                const u32x4 o = *(const u32x4*)(smem + L_STG + (arr * 2560 + mi * 320 + pc * 4) * 4);
                __builtin_amdgcn_raw_buffer_store_b128(o, rs, WS_XS + rank * XS_STR + tid * 16, 0, SC1);
            } else if (tid == 160) {
                __builtin_amdgcn_raw_buffer_store_b64(u32x2{(unsigned)(s0 + 256 * mi), __float_as_uint(rvl[mi])}, rs, WS_XS + rank * XS_STR + 2560, 0, SC1);
            }
            lim = cur;
        }
    }
    STAMP(15);
    unsigned hw = SENT, xw = SENT;
    const int dj = (tid >> 4) & 7, dn = n0 + (tid & 15);
    int xso = 0;
    if (is_down && wave < 2) {
        xso = (nice ? ((const int*)red)[160 + dj] : WS_XS + dj * XS_STR) + (dn >> 1) * 4;
        hw = __builtin_amdgcn_raw_buffer_load_b32(rs, xso, 0, SC1);
        xw = __builtin_amdgcn_raw_buffer_load_b32(rs, xso + 1280, 0, SC1);
    }
    if (wave < 4) {
        const __amdgpu_buffer_rsrc_t ro = __builtin_amdgcn_make_buffer_rsrc(out, 0, 2048 * 640 * 2, 0x00020000);
        const int obase = (s0 + 256 * wave) * 1280;
        const int va_ = lane * 4 + (((selmask >> (wave + 4 * swp)) & 1) << 22), vb_ = lane * 4 + (((selmask >> (wave + 4 - 4 * swp)) & 1) << 22);
#pragma unroll
        for (int i = 0; i < 5; ++i) __builtin_amdgcn_raw_buffer_store_b32(oa[i], ro, va_ + 256 * i, obase + swp * (1024 * 1280), OUT_AUX);
#pragma unroll
        for (int i = 0; i < 5; ++i) __builtin_amdgcn_raw_buffer_store_b32(ob[i], ro, vb_ + 256 * i, obase + (1 - swp) * (1024 * 1280), OUT_AUX);
    }
    if (is_gu) {
        if (!nice) {
            if (wave >= 4) {
                aoff = WS_XS + (lane & 7) * XS_STR + (lane >> 4) * 16 + (wave - 4) * 320; af[0].x = SENT;
                prow = min(ka, 8);
                gu_mfma();
                BAR();
            } else gu_epi(false);
            asm volatile(".Lgu_end:");
        }
    } else if (is_down) {
        u32x2 meta;
        const int j = dj, n = dn;
        const int actl = WS_ACT + (lane >> 4) * 128 + (lane & 7) * 16;
        {
            const unsigned key = ((const unsigned*)red)[144 + j];
            meta = u32x2{~key & 0xffffu, (key & 0x80000000u) ? (key & 0x7fff0000u) : (~key & 0xffff0000u)};
        }
        STAMP(16);
        if (wave >= 4) {
            __builtin_amdgcn_s_sleep(ACT_DELAY);
            auto ld = [&](u32x4 (&a)[14]) {
                CFENCE();
#pragma unroll
                for (int i = 0; i < 14; ++i) a[i] = __builtin_amdgcn_raw_buffer_load_b128(rs, actl + (i & 1) * 2048, i == 13 ? min(wave + 44, 49) * 512 : (wave - 4 + 8 * (i >> 1)) * 512, SC1);
                CFENCE();
            };
            auto ok = [&](const u32x4 (&a)[14]) {
                unsigned m = 0;
#pragma unroll
                for (int i = 0; i < 14; ++i) m = umax3(umax3(m, a[i].x, a[i].y), a[i].z, a[i].w);
                return (bool)__all(m != SENT);
            };
            auto fin = [&](const u32x4 (&a)[14]) {
                STAMP(18);
                f32x4 acc[2] = {{0.f, 0.f, 0.f, 0.f}, {0.f, 0.f, 0.f, 0.f}};
#pragma unroll
                for (int i = 0; i < 13; ++i)
                    acc[i & 1] = __builtin_amdgcn_mfma_f32_16x16x32_bf16(__builtin_bit_cast(bf16x8_t, bfr[i]), __builtin_bit_cast(bf16x8_t, a[i]), acc[i & 1], 0, 0, 0);
                if (wave < 6)
                    acc[1] = __builtin_amdgcn_mfma_f32_16x16x32_bf16(__builtin_bit_cast(bf16x8_t, bfr[13]), __builtin_bit_cast(bf16x8_t, a[13]), acc[1], 0, 0, 0);
                if (ka < 8) *(f32x4*)(part + ((wave - 4) * 8 + ka) * 16 + g4 * 4) = acc[0] + acc[1];
            };
            u32x4 ag[14];
            for (int spin = 0; spin < SPIN_CAP; ++spin) { ld(ag); if (ok(ag)) break; }
            fin(ag);
        }
        if (wave < 2) {
            if (!nice) meta.x = SENT;
#pragma unroll 1
            for (int spin = 0; spin < SPIN_CAP; ++spin) {
                if (__all(umax3(hw, xw, meta.x) != SENT)) break;
                CFENCE();
                hw = __builtin_amdgcn_raw_buffer_load_b32(rs, xso, 0, SC1);
                xw = __builtin_amdgcn_raw_buffer_load_b32(rs, xso + 1280, 0, SC1);
                if (!nice) meta = __builtin_amdgcn_raw_buffer_load_b64(rs, WS_XS + j * XS_STR + 2560, 0, SC1);
            }
            float sg = rbf(__builtin_amdgcn_rcpf(1.0f + __expf(-__uint_as_float(meta.y))));
            float h2 = (n & 1) ? bfhi(hw) : bf2f(hw), x1 = (n & 1) ? bfhi(xw) : bf2f(xw);
            float gb = ((const float*)(smem + L_MB))[1920 + n];
            unsigned oo = (min(meta.x, 2047u) * 640u + (unsigned)n) * 2u;
            PIN(sg); PIN(h2); PIN(x1); PIN(gb); PIN(oo);
            BAR();
            float y = (part[tid] + part[128 + tid]) + (part[256 + tid] + part[384 + tid]);
            y = rbf(rbf(y) * sg);
            const float o = x1 + rbf(gb * rbf(h2 + y));
            *(u16*)((unsigned char*)out + oo) = (u16)(pk_bf16(c32{o, o}) & 0xffffu);
        } else BAR();
        STAMP(19);
        asm volatile(".Ldn_end:");
    }
    if (b == 0 && tid == 0) *(volatile unsigned*)(ws + WS_EPOCH) = tag;
    STAMP(20);
    asm volatile(".Lfused_end:");
}

extern "C" void kernel_launch(void* const* d_in, const int* in_sizes, int n_in,
                              void* d_out, int out_size, void* d_ws, size_t ws_size,
                              hipStream_t stream) {
    (void)hipFuncSetAttribute((const void*)fused, hipFuncAttributeMaxDynamicSharedMemorySize, L_TOTAL);
    fused<<<256, 512, L_TOTAL, stream>>>((const u16*)d_in[9], (const u16*)d_in[8], (const u16*)d_in[0], (unsigned char*)d_ws,
                                         (const u16*)d_in[1], (const u16*)d_in[2], (const u16*)d_in[4], (u16*)d_out,
                                         (const u16*)d_in[5], (const u16*)d_in[6], (const u16*)d_in[7]);
}
```
